```python
import torch

def _softmax(x, axis=-1):
    return torch.softmax(x, dim=axis)
def _gelu(x):
    return torch.nn.functional.gelu(x, approximate="tanh")
def _silu(x):
    return torch.nn.functional.silu(x)
def _sigmoid(x):
    return torch.sigmoid(x)
def _layernorm(x, axis=-1, eps=1e-5):
    m = x.mean(dim=axis, keepdim=True)
    v = x.var(dim=axis, keepdim=True, unbiased=False)
    return (x - m) / torch.sqrt(v + eps)
def _scatter_add(tgt, idx, src, axis=0):
    return tgt.clone().index_add_(axis, idx, src)
def _segment_sum(src, seg, n):
    out = torch.zeros((n,) + src.shape[1:], dtype=src.dtype)
    return out.index_add_(0, seg, src)
def _pad(x, pw):
    flat = tuple(v for pair in reversed(pw) for v in pair)
    return torch.nn.functional.pad(x, flat)

kernel_name = 'standalone__cheap_equivariant_egnn__graph'

def setup_inputs():

    _w = (lambda a: (lambda t: t.to(torch.bfloat16) if t.is_floating_point() else t)(torch.as_tensor(a)))
    N, deg, D, De = 10000, 16, 128, max(0, 1)
    E = N * deg
    return {
        "h": _w((lambda *s: torch.randn(*s, dtype=torch.bfloat16) * 0.2)(N, D)),
        "pos": _w((lambda *s: torch.randn(*s, dtype=torch.bfloat16) * 0.8)(N, 3)),
        "e_attr": _w((lambda *s: torch.randn(*s, dtype=torch.bfloat16) * 0.8)(E, De)),
        "src": _w((lambda lo, hi, *s: torch.randint(lo, hi, s))(0, N, E).to(torch.int64)),
        "dst": _w((lambda lo, hi, *s: torch.randint(lo, hi, s))(0, N, E).to(torch.int64)),
        "w_e": _w(0.1 * (lambda *s: torch.randn(*s, dtype=torch.bfloat16) * 0.8)(2 * D + 1 + De, D)),
        "w_x": _w(0.1 * (lambda *s: torch.randn(*s, dtype=torch.bfloat16) * 0.2)(D, 1)),
        "w_h": _w(0.1 * (lambda *s: torch.randn(*s, dtype=torch.bfloat16) * 0.2)(2 * D, D)),
    }

def reference(h, pos, e_attr, src, dst, w_e, w_x, w_h):
    N, D = h.shape[0], h.shape[1]
    diff = pos[dst] - pos[src]
    dist2 = torch.sum(diff * diff, axis=-1, keepdims=True)
    e_in = torch.concatenate([h[dst], h[src], dist2, e_attr], axis=-1)
    m_ij = _silu(e_in @ w_e)
    coord_scale = m_ij @ w_x
    pos_new = pos + _segment_sum(diff * coord_scale, dst, N)
    m_i = _segment_sum(m_ij, dst, N)
    h_new = h + _silu(torch.concatenate([h, m_i], axis=-1) @ w_h)
    return h_new, pos_new
```

```cpp
#include <hip/hip_runtime.h>
#include <hip/hip_bf16.h>
#include <stdint.h>

static __device__ __forceinline__ float bf2f(unsigned short u){ return __uint_as_float(((unsigned)u)<<16); }
static __device__ __forceinline__ unsigned short f2bf(float f){ __hip_bfloat16 b = __float2bfloat16(f); return *reinterpret_cast<unsigned short*>(&b); }
static __device__ __forceinline__ float rbf(float f){ return bf2f(f2bf(f)); }

__global__ void k_pre(const unsigned short* h, const unsigned short* we, float* AB, int N){
  int n = blockIdx.x; int c = threadIdx.x;
  __shared__ float hs[128];
  if (c<128) hs[c]=bf2f(h[n*128+c]);
  __syncthreads();
  int half = c>>7, col = c&127;
  float acc=0.f;
  for(int k=0;k<128;k++) acc += hs[k]*bf2f(we[(half*128+k)*128+col]);
  AB[n*256+c]=acc;
}
__global__ void k_edge(const float* AB, const unsigned short* pos, const unsigned short* ea, const int* src, const int* dst,
   const unsigned short* we, const unsigned short* wx, float* mi, float* pacc, int E){
  int e = blockIdx.x*4 + (threadIdx.x>>6); int l = threadIdx.x&63;
  if(e>=E) return;
  int s=src[e], d=dst[e];
  float df[3]; float d2=0.f;
  for(int i=0;i<3;i++){ df[i]=rbf(bf2f(pos[d*3+i])-bf2f(pos[s*3+i])); d2+=rbf(df[i]*df[i]); }
  d2=rbf(d2);
  float a=bf2f(ea[e]);
  float t=0.f;
  for(int j=0;j<2;j++){
    int c=l+64*j;
    float x=AB[d*256+c]+AB[s*256+128+c]+d2*bf2f(we[256*128+c])+a*bf2f(we[257*128+c]);
    float m=x/(1.f+__expf(-x));
    m=rbf(m);
    atomicAdd(&mi[d*128+c], m);
    t+=m*bf2f(wx[c]);
  }
  for(int o=32;o>0;o>>=1) t+=__shfl_xor(t,o);
  if(l<3) atomicAdd(&pacc[d*3+l], df[l]*t);
}
__global__ void k_node(const unsigned short* h, const unsigned short* pos, const float* mi, const float* pacc, const unsigned short* wh, unsigned short* out, int N){
  int n=blockIdx.x; int c=threadIdx.x;
  __shared__ float xs[256];
  xs[c]=bf2f(h[n*128+c]); xs[128+c]=rbf(mi[n*128+c]);
  __syncthreads();
  float acc=0.f;
  for(int k=0;k<256;k++) acc+=xs[k]*bf2f(wh[k*128+c]);
  float s=acc/(1.f+__expf(-acc));
  out[n*128+c]=f2bf(xs[c]+s);
  if(c<3) out[N*128+n*3+c]=f2bf(bf2f(pos[n*3+c])+pacc[n*3+c]);
}

extern "C" void kernel_launch(void* const* d_in, const int* in_sizes, int n_in,
                              void* d_out, int out_size, void* d_ws, size_t ws_size,
                              hipStream_t stream) {
  const unsigned short* h=(const unsigned short*)d_in[0];
  const unsigned short* pos=(const unsigned short*)d_in[1];
  const unsigned short* ea=(const unsigned short*)d_in[2];
  const int* src=(const int*)d_in[3];
  const int* dst=(const int*)d_in[4];
  const unsigned short* we=(const unsigned short*)d_in[5];
  const unsigned short* wx=(const unsigned short*)d_in[6];
  const unsigned short* wh=(const unsigned short*)d_in[7];
  int N=in_sizes[0]/128, E=in_sizes[3];
  float* AB=(float*)d_ws; float* mi=AB+(size_t)N*256; float* pacc=mi+(size_t)N*128;
  hipMemsetAsync(mi,0,(size_t)N*(128+4)*4,stream);
  k_pre<<<N,256,0,stream>>>(h,we,AB,N);
  k_edge<<<(E+3)/4,256,0,stream>>>(AB,pos,ea,src,dst,we,wx,mi,pacc,E);
  k_node<<<N,128,0,stream>>>(h,pos,mi,pacc,wh,(unsigned short*)d_out,N);
}
```

```cpp
#include <hip/hip_runtime.h>
#include <hip/hip_bf16.h>
#include <stdint.h>

typedef unsigned short u16;
typedef unsigned int u32;
typedef __attribute__((ext_vector_type(8))) short bf16x8;
typedef __attribute__((ext_vector_type(4))) short s16x4;
typedef __attribute__((ext_vector_type(16))) float f32x16;
typedef __attribute__((ext_vector_type(4))) float f32x4;
typedef __attribute__((ext_vector_type(2))) float f32x2;
typedef __attribute__((ext_vector_type(2))) __bf16 bf16x2_t;
typedef __attribute__((ext_vector_type(4))) u32 u32x4;
typedef __attribute__((ext_vector_type(2))) u32 u32x2;
typedef __attribute__((ext_vector_type(4))) int i32x4;
#define LDSP __attribute__((address_space(3)))
#define DI __device__ __forceinline__

constexpr int NN = 10000, EE = 160000;
constexpr int NBIN = 256, NPROD = 64, CHUNK = 2500, SLOTW = 32, CAP = 31;
constexpr int NPACKB = 16;
constexpr size_t WS_SLOTS = 0;
constexpr size_t WS_W1F = (size_t)NBIN * NPROD * SLOTW * 4;
constexpr size_t WS_WHF = WS_W1F + 32768;
constexpr size_t WS_BP = WS_WHF + 65536;
constexpr size_t WS_STAMP = 8u << 20;
constexpr float CL2 = -1.4426950408889634f;
constexpr int LCAP = 1024;

constexpr int MP = 272, AP = 264;
constexpr int L_MIMG = 0;
constexpr int L_HT = L_MIMG + 40 * MP;
constexpr int L_RECA = L_HT + 48 * 256;
constexpr int L_RECB = L_RECA + 8 * LCAP;
constexpr int L_LIST = L_RECB + 4 * LCAP;
constexpr int L_NL = L_LIST + 4 * LCAP;
constexpr int L_POS = L_NL + LCAP;
constexpr int L_WX = L_POS + 40 * 16;
constexpr int L_CNT = L_WX + 512;
constexpr int L_AIMG = L_CNT + 256;
constexpr int L_S = (L_AIMG + 53 * AP + 15) / 16 * 16;
constexpr int L_ROWS = L_S + 8 * 2560;
constexpr int L_TOTAL = L_ROWS + 65536;
static_assert(L_S <= 65536, "lds");
static_assert(L_TOTAL <= 163840, "lds");

DI float bf2f(u32 u) { return __uint_as_float(u << 16); }
DI u32 pk_bf16(float a, float b) { f32x2 v = {a, b}; return __builtin_bit_cast(u32, __builtin_convertvector(v, bf16x2_t)); }
DI float lo_f(u32 p) { return __uint_as_float(p << 16); }
DI float hi_f(u32 p) { return __uint_as_float(p & 0xffff0000u); }
DI int node0_of(int b) { return (625 * b + 15) >> 4; }

#define STAMP_DECL
#define TICK0
#define TICK(k)
#define STAMP_OUT
#define STAMP(i) do {} while (0)

#define MFMA32(a, b, c) __builtin_amdgcn_mfma_f32_32x32x16_bf16((a), (b), (c), 0, 0, 0)
#define MFMA16(a, b, c) __builtin_amdgcn_mfma_f32_16x16x32_bf16((a), (b), (c), 0, 0, 0)
template <typename T> DI T lds_ld(char* lds, int off) { return *(T*)(lds + off); }
template <typename T> DI void lds_st(char* lds, int off, T v) { *(T*)(lds + off) = v; }
DI s16x4 lds_tr(char* lds, int off) { return __builtin_amdgcn_ds_read_tr16_b64_v4i16((LDSP s16x4*)(lds + off)); }
DI bf16x8 cat4(s16x4 a, s16x4 b) { return __builtin_shufflevector(a, b, 0, 1, 2, 3, 4, 5, 6, 7); }
DI void wg_barrier() { asm volatile("s_waitcnt lgkmcnt(0)\n\ts_barrier" ::: "memory"); }

DI void gld16(u32x4& d, const void* sbase, u32 voff) { asm volatile("global_load_dwordx4 %0, %1, %2" : "=v"(d) : "v"(voff), "s"(sbase) : "memory"); }
template <int OFF> DI void gld16o(u32x4& d, const void* sbase, u32 voff) { asm volatile("global_load_dwordx4 %0, %1, %2 offset:%3" : "=v"(d) : "v"(voff), "s"(sbase), "i"(OFF) : "memory"); }
DI void gld8(u32x2& d, const void* sbase, u32 voff) { asm volatile("global_load_dwordx2 %0, %1, %2" : "=v"(d) : "v"(voff), "s"(sbase) : "memory"); }
DI void gld2(u32& d, const void* sbase, u32 voff) { asm volatile("global_load_ushort %0, %1, %2" : "=v"(d) : "v"(voff), "s"(sbase) : "memory"); }
DI void gld2p(u32& d, const void* p) { asm volatile("global_load_ushort %0, %1, off" : "=v"(d) : "v"(p) : "memory"); }

DI void dma1(const void* gbase, u32 voff, u32 ldsb) {
  u32 keep;
  asm volatile("s_mov_b32 %0, m0\n\ts_mov_b32 m0, %3\n\ts_nop 0\n\tglobal_load_lds_dwordx4 %1, %2\n\ts_mov_b32 m0, %0"
               : "=&s"(keep) : "v"(voff), "s"(gbase), "s"(ldsb) : "memory");
}
DI void dma2(const void* gbase, u32 voff, u32 ldsb) {
  u32 keep;
  asm volatile("s_mov_b32 %0, m0\n\ts_mov_b32 m0, %3\n\ts_nop 0\n\tglobal_load_lds_dwordx4 %1, %2\n\t"
               "global_load_lds_dwordx4 %1, %2 offset:1024\n\ts_mov_b32 m0, %0"
               : "=&s"(keep) : "v"(voff), "s"(gbase), "s"(ldsb) : "memory");
}
DI void dma_rows(const void* gbase, u32 v0, u32 v1, u32 v2, u32 v3, u32 ldsb) {
  u32 keep;
  asm volatile(
      "s_mov_b32 %0, m0\n\t"
      "s_mov_b32 m0, %6\n\ts_nop 0\n\tglobal_load_lds_dwordx4 %1, %5\n\t"
      "s_add_u32 m0, %6, 3968\n\ts_nop 0\n\tglobal_load_lds_dwordx4 %1, %5 offset:128\n\t"
      "s_add_u32 m0, %6, 1024\n\ts_nop 0\n\tglobal_load_lds_dwordx4 %2, %5\n\t"
      "s_add_u32 m0, %6, 4992\n\ts_nop 0\n\tglobal_load_lds_dwordx4 %2, %5 offset:128\n\t"
      "s_add_u32 m0, %6, 2048\n\ts_nop 0\n\tglobal_load_lds_dwordx4 %3, %5\n\t"
      "s_add_u32 m0, %6, 6016\n\ts_nop 0\n\tglobal_load_lds_dwordx4 %3, %5 offset:128\n\t"
      "s_add_u32 m0, %6, 3072\n\ts_nop 0\n\tglobal_load_lds_dwordx4 %4, %5\n\t"
      "s_add_u32 m0, %6, 7040\n\ts_nop 0\n\tglobal_load_lds_dwordx4 %4, %5 offset:128\n\t"
      "s_mov_b32 m0, %0"
      : "=&s"(keep)
      : "v"(v0), "v"(v1), "v"(v2), "v"(v3), "s"(gbase), "s"(ldsb)
      : "memory", "scc");
}

DI void stage_h_tile(const u16* h, int n0, int nn, int w, int lane, u32 ldsdst) {
  if (w < 6) {
#pragma unroll
    for (int i = 0; i < 2; ++i) {
      const int row = 8 * w + 4 * i + (lane >> 4);
      const u32 voff = (u32)(n0 + min(row, nn - 1)) * 256u + (u32)(((lane & 15) ^ (row & 15)) * 16);
      dma1(h, voff, ldsdst + (8 * w + 4 * i) * 256);
    }
  }
}

__global__ __launch_bounds__(512) void k_pre(const int* __restrict__ src, const int* __restrict__ dst, const u16* __restrict__ h,
                                             const u16* __restrict__ we, const u16* __restrict__ wh, char* __restrict__ ws) {
  __shared__ __attribute__((aligned(16))) char lds[32768 + 12288];
  const int tid = threadIdx.x, lane = tid & 63, p = blockIdx.x;
  if (p < NPROD) {
    u32* cnt = (u32*)lds;
    u32* slots = (u32*)(ws + WS_SLOTS);
    const int q = p * (CHUNK / 4) + tid;
    const bool two = tid < CHUNK / 4 - 512;
    const i32x4 d0 = ((const i32x4*)dst)[q];
    const i32x4 s0 = ((const i32x4*)src)[q];
    i32x4 d1 = {0, 0, 0, 0}, s1 = {0, 0, 0, 0};
    if (two) { d1 = ((const i32x4*)dst)[q + 512]; s1 = ((const i32x4*)src)[q + 512]; }
    if (tid < NBIN) cnt[tid] = 0;
    __syncthreads();
#pragma unroll
    for (int j = 0; j < 8; ++j) {
      if (j < 4 || two) {
        const u32 n = (u32)(j < 4 ? d0[j & 3] : d1[j & 3]);
        const u32 sv = (u32)(j < 4 ? s0[j & 3] : s1[j & 3]);
        const u32 bin = (n * 16u) / 625u;
        const u32 nl = n - ((625u * bin + 15u) >> 4);
        const u32 rank = atomicAdd(&cnt[bin], 1u);
        if (rank < CAP) slots[(bin * NPROD + p) * SLOTW + 1 + rank] = sv | (nl << 14) | ((u32)((tid + (j < 4 ? 0 : 512)) * 4 + (j & 3)) << 20);
      }
    }
    __syncthreads();
    if (tid < NBIN) slots[(tid * NPROD + p) * SLOTW] = cnt[tid];
  } else if (p < NPROD + NPACKB) {
    const int f = (p - NPROD) * 512 + tid;
    if (f < 6144) {
      const int l = f & 63;
      const u16* g;
      if (f < 2048) { const int s = (f >> 6) & 3, t = f >> 8; g = we + (32 * s + 8 * (l >> 4)) * 128 + 16 * t + (l & 15); }
      else { const int f3 = f - 2048; const int s = (f3 >> 6) & 7, t = f3 >> 9; g = wh + (32 * s + 8 * (l >> 4)) * 128 + 16 * t + (l & 15); }
      u32 v[8];
      u32x4 o;
#pragma unroll
      for (int j = 0; j < 8; ++j) v[j] = g[j * 128];
#pragma unroll
      for (int j = 0; j < 4; ++j) o[j] = v[2 * j] | (v[2 * j + 1] << 16);
      *(u32x4*)(ws + WS_W1F + (size_t)f * 16) = o;
    }
  } else {
    const int b = p - NPROD - NPACKB;
    const int w = __builtin_amdgcn_readfirstlane(tid >> 6);
    const int n0 = node0_of(b), nn = node0_of(b + 1) - n0;
    const u32 ldsbase = (u32)(size_t)(LDSP char*)lds;
    const int l15 = lane & 15, g4 = lane >> 4;
    stage_h_tile(h, n0, nn, w, lane, ldsbase + 32768);
    const u32 vb = (u32)((128 + 16 * w + g4) * 256);
    dma2(we, vb + (u32)((l15 ^ (g4 << 1)) * 16), ldsbase + w * 4096);
    dma2(we, vb + 2048u + (u32)((l15 ^ (g4 << 1) ^ 8) * 16), ldsbase + w * 4096 + 2048);
    asm volatile("s_waitcnt vmcnt(0)" ::: "memory");
    __syncthreads();
    const int tq = l15 >> 2, tp = l15 & 3;
    const int wa = (8 * g4 + tq) * 256 + (((2 * w + (tp >> 1)) ^ ((tq | ((g4 & 1) << 2)) << 1)) * 16) + (tp & 1) * 8;
    bf16x8 wf[4];
#pragma unroll
    for (int s = 0; s < 4; ++s) wf[s] = cat4(lds_tr(lds, wa + s * 8192), lds_tr(lds, wa + s * 8192 + 1024));
    u16* bp = (u16*)(ws + WS_BP);
#pragma unroll
    for (int nt = 0; nt < 3; ++nt) {
      f32x4 a = {0.f, 0.f, 0.f, 0.f};
#pragma unroll
      for (int s = 0; s < 4; ++s) {
        const bf16x8 hb = lds_ld<bf16x8>(lds, 32768 + (16 * nt + l15) * 256 + (((4 * s + g4) ^ l15) * 16));
        a = MFMA16(wf[s], hb, a);
      }
      if (16 * nt + l15 < nn) {
        const u32x2 o = {pk_bf16(a[0] * CL2, a[1] * CL2), pk_bf16(a[2] * CL2, a[3] * CL2)};
        *(u32x2*)(bp + (size_t)(n0 + 16 * nt + l15) * 128 + 16 * w + 4 * g4) = o;
      }
    }
  }
}

#define BLK_ASM \
  "s_cmp_gt_i32 %22, 24\n" \
  "s_cbranch_scc0 .Lblk3_%=\n" \
  "v_mfma_f32_32x32x16_bf16 v[192:207], %14, %17, 0\n" \
  "v_mfma_f32_32x32x16_bf16 v[192:207], %15, %4, v[192:207]\n" \
  "v_mfma_f32_32x32x16_bf16 v[192:207], %16, %5, v[192:207]\n" \
  "s_nop 11\n" \
  "v_exp_f32 v224, v192\n" \
  "v_exp_f32 v225, v193\n" \
  "v_exp_f32 v226, v194\n" \
  "v_exp_f32 v227, v195\n" \
  "v_exp_f32 v228, v196\n" \
  "v_exp_f32 v229, v197\n" \
  "v_exp_f32 v230, v198\n" \
  "v_exp_f32 v231, v199\n" \
  "v_exp_f32 v232, v200\n" \
  "v_mfma_f32_32x32x16_bf16 v[208:223], %14, %18, 0\n" \
  "v_exp_f32 v233, v201\n" \
  "v_exp_f32 v234, v202\n" \
  "v_exp_f32 v235, v203\n" \
  "v_mfma_f32_32x32x16_bf16 v[208:223], %15, %6, v[208:223]\n" \
  "v_exp_f32 v236, v204\n" \
  "v_exp_f32 v237, v205\n" \
  "v_exp_f32 v238, v206\n" \
  "v_exp_f32 v239, v207\n" \
  "v_pk_fma_f32 v[224:225], v[224:225], %21, %21\n" \
  "v_pk_fma_f32 v[226:227], v[226:227], %21, %21\n" \
  "v_pk_fma_f32 v[228:229], v[228:229], %21, %21\n" \
  "v_pk_fma_f32 v[230:231], v[230:231], %21, %21\n" \
  "v_pk_fma_f32 v[232:233], v[232:233], %21, %21\n" \
  "v_pk_fma_f32 v[234:235], v[234:235], %21, %21\n" \
  "v_pk_fma_f32 v[236:237], v[236:237], %21, %21\n" \
  "v_pk_fma_f32 v[238:239], v[238:239], %21, %21\n" \
  "v_mfma_f32_32x32x16_bf16 v[208:223], %16, %7, v[208:223]\n" \
  "v_rcp_f32 v224, v224\n" \
  "v_rcp_f32 v225, v225\n" \
  "v_rcp_f32 v226, v226\n" \
  "v_rcp_f32 v227, v227\n" \
  "v_rcp_f32 v228, v228\n" \
  "v_rcp_f32 v229, v229\n" \
  "v_rcp_f32 v230, v230\n" \
  "v_rcp_f32 v231, v231\n" \
  "v_rcp_f32 v232, v232\n" \
  "v_rcp_f32 v233, v233\n" \
  "v_rcp_f32 v234, v234\n" \
  "v_rcp_f32 v235, v235\n" \
  "v_rcp_f32 v236, v236\n" \
  "v_rcp_f32 v237, v237\n" \
  "v_rcp_f32 v238, v238\n" \
  "v_rcp_f32 v239, v239\n" \
  "v_pk_mul_f32 v[224:225], v[224:225], v[192:193]\n" \
  "v_pk_mul_f32 v[226:227], v[226:227], v[194:195]\n" \
  "v_pk_mul_f32 v[228:229], v[228:229], v[196:197]\n" \
  "v_pk_mul_f32 v[230:231], v[230:231], v[198:199]\n" \
  "v_pk_mul_f32 v[232:233], v[232:233], v[200:201]\n" \
  "v_pk_mul_f32 v[234:235], v[234:235], v[202:203]\n" \
  "v_pk_mul_f32 v[236:237], v[236:237], v[204:205]\n" \
  "v_pk_mul_f32 v[238:239], v[238:239], v[206:207]\n" \
  "v_cvt_pk_bf16_f32 v240, v224, v225\n" \
  "v_cvt_pk_bf16_f32 v241, v226, v227\n" \
  "v_cvt_pk_bf16_f32 v242, v228, v229\n" \
  "v_cvt_pk_bf16_f32 v243, v230, v231\n" \
  "v_cvt_pk_bf16_f32 v244, v232, v233\n" \
  "v_cvt_pk_bf16_f32 v245, v234, v235\n" \
  "v_cvt_pk_bf16_f32 v246, v236, v237\n" \
  "v_cvt_pk_bf16_f32 v247, v238, v239\n" \
  "v_exp_f32 v224, v208\n" \
  "v_exp_f32 v225, v209\n" \
  "v_exp_f32 v226, v210\n" \
  "v_mfma_f32_32x32x16_bf16 %0, v[240:243], %12, %0\n" \
  "v_exp_f32 v227, v211\n" \
  "v_exp_f32 v228, v212\n" \
  "v_exp_f32 v229, v213\n" \
  "v_mfma_f32_32x32x16_bf16 %0, v[244:247], %13, %0\n" \
  "v_exp_f32 v230, v214\n" \
  "v_exp_f32 v231, v215\n" \
  "v_exp_f32 v232, v216\n" \
  "v_mfma_f32_32x32x16_bf16 v[192:207], %14, %19, 0\n" \
  "v_exp_f32 v233, v217\n" \
  "v_exp_f32 v234, v218\n" \
  "v_exp_f32 v235, v219\n" \
  "v_mfma_f32_32x32x16_bf16 v[192:207], %15, %8, v[192:207]\n" \
  "v_exp_f32 v236, v220\n" \
  "v_exp_f32 v237, v221\n" \
  "v_exp_f32 v238, v222\n" \
  "v_exp_f32 v239, v223\n" \
  "v_pk_fma_f32 v[224:225], v[224:225], %21, %21\n" \
  "v_pk_fma_f32 v[226:227], v[226:227], %21, %21\n" \
  "v_pk_fma_f32 v[228:229], v[228:229], %21, %21\n" \
  "v_pk_fma_f32 v[230:231], v[230:231], %21, %21\n" \
  "v_pk_fma_f32 v[232:233], v[232:233], %21, %21\n" \
  "v_pk_fma_f32 v[234:235], v[234:235], %21, %21\n" \
  "v_pk_fma_f32 v[236:237], v[236:237], %21, %21\n" \
  "v_pk_fma_f32 v[238:239], v[238:239], %21, %21\n" \
  "v_mfma_f32_32x32x16_bf16 v[192:207], %16, %9, v[192:207]\n" \
  "v_rcp_f32 v224, v224\n" \
  "v_rcp_f32 v225, v225\n" \
  "v_rcp_f32 v226, v226\n" \
  "v_rcp_f32 v227, v227\n" \
  "v_rcp_f32 v228, v228\n" \
  "v_rcp_f32 v229, v229\n" \
  "v_rcp_f32 v230, v230\n" \
  "v_rcp_f32 v231, v231\n" \
  "v_rcp_f32 v232, v232\n" \
  "v_rcp_f32 v233, v233\n" \
  "v_rcp_f32 v234, v234\n" \
  "v_rcp_f32 v235, v235\n" \
  "v_rcp_f32 v236, v236\n" \
  "v_rcp_f32 v237, v237\n" \
  "v_rcp_f32 v238, v238\n" \
  "v_rcp_f32 v239, v239\n" \
  "v_pk_mul_f32 v[224:225], v[224:225], v[208:209]\n" \
  "v_pk_mul_f32 v[226:227], v[226:227], v[210:211]\n" \
  "v_pk_mul_f32 v[228:229], v[228:229], v[212:213]\n" \
  "v_pk_mul_f32 v[230:231], v[230:231], v[214:215]\n" \
  "v_pk_mul_f32 v[232:233], v[232:233], v[216:217]\n" \
  "v_pk_mul_f32 v[234:235], v[234:235], v[218:219]\n" \
  "v_pk_mul_f32 v[236:237], v[236:237], v[220:221]\n" \
  "v_pk_mul_f32 v[238:239], v[238:239], v[222:223]\n" \
  "v_cvt_pk_bf16_f32 v240, v224, v225\n" \
  "v_cvt_pk_bf16_f32 v241, v226, v227\n" \
  "v_cvt_pk_bf16_f32 v242, v228, v229\n" \
  "v_cvt_pk_bf16_f32 v243, v230, v231\n" \
  "v_cvt_pk_bf16_f32 v244, v232, v233\n" \
  "v_cvt_pk_bf16_f32 v245, v234, v235\n" \
  "v_cvt_pk_bf16_f32 v246, v236, v237\n" \
  "v_cvt_pk_bf16_f32 v247, v238, v239\n" \
  "v_exp_f32 v224, v192\n" \
  "v_exp_f32 v225, v193\n" \
  "v_exp_f32 v226, v194\n" \
  "v_mfma_f32_32x32x16_bf16 %1, v[240:243], %12, %1\n" \
  "v_exp_f32 v227, v195\n" \
  "v_exp_f32 v228, v196\n" \
  "v_exp_f32 v229, v197\n" \
  "v_mfma_f32_32x32x16_bf16 %1, v[244:247], %13, %1\n" \
  "v_exp_f32 v230, v198\n" \
  "v_exp_f32 v231, v199\n" \
  "v_exp_f32 v232, v200\n" \
  "v_mfma_f32_32x32x16_bf16 v[208:223], %14, %20, 0\n" \
  "v_exp_f32 v233, v201\n" \
  "v_exp_f32 v234, v202\n" \
  "v_exp_f32 v235, v203\n" \
  "v_mfma_f32_32x32x16_bf16 v[208:223], %15, %10, v[208:223]\n" \
  "v_exp_f32 v236, v204\n" \
  "v_exp_f32 v237, v205\n" \
  "v_exp_f32 v238, v206\n" \
  "v_exp_f32 v239, v207\n" \
  "v_pk_fma_f32 v[224:225], v[224:225], %21, %21\n" \
  "v_pk_fma_f32 v[226:227], v[226:227], %21, %21\n" \
  "v_pk_fma_f32 v[228:229], v[228:229], %21, %21\n" \
  "v_pk_fma_f32 v[230:231], v[230:231], %21, %21\n" \
  "v_pk_fma_f32 v[232:233], v[232:233], %21, %21\n" \
  "v_pk_fma_f32 v[234:235], v[234:235], %21, %21\n" \
  "v_pk_fma_f32 v[236:237], v[236:237], %21, %21\n" \
  "v_pk_fma_f32 v[238:239], v[238:239], %21, %21\n" \
  "v_mfma_f32_32x32x16_bf16 v[208:223], %16, %11, v[208:223]\n" \
  "v_rcp_f32 v224, v224\n" \
  "v_rcp_f32 v225, v225\n" \
  "v_rcp_f32 v226, v226\n" \
  "v_rcp_f32 v227, v227\n" \
  "v_rcp_f32 v228, v228\n" \
  "v_rcp_f32 v229, v229\n" \
  "v_rcp_f32 v230, v230\n" \
  "v_rcp_f32 v231, v231\n" \
  "v_rcp_f32 v232, v232\n" \
  "v_rcp_f32 v233, v233\n" \
  "v_rcp_f32 v234, v234\n" \
  "v_rcp_f32 v235, v235\n" \
  "v_rcp_f32 v236, v236\n" \
  "v_rcp_f32 v237, v237\n" \
  "v_rcp_f32 v238, v238\n" \
  "v_rcp_f32 v239, v239\n" \
  "v_pk_mul_f32 v[224:225], v[224:225], v[192:193]\n" \
  "v_pk_mul_f32 v[226:227], v[226:227], v[194:195]\n" \
  "v_pk_mul_f32 v[228:229], v[228:229], v[196:197]\n" \
  "v_pk_mul_f32 v[230:231], v[230:231], v[198:199]\n" \
  "v_pk_mul_f32 v[232:233], v[232:233], v[200:201]\n" \
  "v_pk_mul_f32 v[234:235], v[234:235], v[202:203]\n" \
  "v_pk_mul_f32 v[236:237], v[236:237], v[204:205]\n" \
  "v_pk_mul_f32 v[238:239], v[238:239], v[206:207]\n" \
  "v_cvt_pk_bf16_f32 v240, v224, v225\n" \
  "v_cvt_pk_bf16_f32 v241, v226, v227\n" \
  "v_cvt_pk_bf16_f32 v242, v228, v229\n" \
  "v_cvt_pk_bf16_f32 v243, v230, v231\n" \
  "v_cvt_pk_bf16_f32 v244, v232, v233\n" \
  "v_cvt_pk_bf16_f32 v245, v234, v235\n" \
  "v_cvt_pk_bf16_f32 v246, v236, v237\n" \
  "v_cvt_pk_bf16_f32 v247, v238, v239\n" \
  "v_exp_f32 v224, v208\n" \
  "v_exp_f32 v225, v209\n" \
  "v_exp_f32 v226, v210\n" \
  "v_mfma_f32_32x32x16_bf16 %2, v[240:243], %12, %2\n" \
  "v_exp_f32 v227, v211\n" \
  "v_exp_f32 v228, v212\n" \
  "v_exp_f32 v229, v213\n" \
  "v_mfma_f32_32x32x16_bf16 %2, v[244:247], %13, %2\n" \
  "v_exp_f32 v230, v214\n" \
  "v_exp_f32 v231, v215\n" \
  "v_exp_f32 v232, v216\n" \
  "v_exp_f32 v233, v217\n" \
  "v_exp_f32 v234, v218\n" \
  "v_exp_f32 v235, v219\n" \
  "v_exp_f32 v236, v220\n" \
  "v_exp_f32 v237, v221\n" \
  "v_exp_f32 v238, v222\n" \
  "v_exp_f32 v239, v223\n" \
  "v_pk_fma_f32 v[224:225], v[224:225], %21, %21\n" \
  "v_pk_fma_f32 v[226:227], v[226:227], %21, %21\n" \
  "v_pk_fma_f32 v[228:229], v[228:229], %21, %21\n" \
  "v_pk_fma_f32 v[230:231], v[230:231], %21, %21\n" \
  "v_pk_fma_f32 v[232:233], v[232:233], %21, %21\n" \
  "v_pk_fma_f32 v[234:235], v[234:235], %21, %21\n" \
  "v_pk_fma_f32 v[236:237], v[236:237], %21, %21\n" \
  "v_pk_fma_f32 v[238:239], v[238:239], %21, %21\n" \
  "v_rcp_f32 v224, v224\n" \
  "v_rcp_f32 v225, v225\n" \
  "v_rcp_f32 v226, v226\n" \
  "v_rcp_f32 v227, v227\n" \
  "v_rcp_f32 v228, v228\n" \
  "v_rcp_f32 v229, v229\n" \
  "v_rcp_f32 v230, v230\n" \
  "v_rcp_f32 v231, v231\n" \
  "v_rcp_f32 v232, v232\n" \
  "v_rcp_f32 v233, v233\n" \
  "v_rcp_f32 v234, v234\n" \
  "v_rcp_f32 v235, v235\n" \
  "v_rcp_f32 v236, v236\n" \
  "v_rcp_f32 v237, v237\n" \
  "v_rcp_f32 v238, v238\n" \
  "v_rcp_f32 v239, v239\n" \
  "v_pk_mul_f32 v[224:225], v[224:225], v[208:209]\n" \
  "v_pk_mul_f32 v[226:227], v[226:227], v[210:211]\n" \
  "v_pk_mul_f32 v[228:229], v[228:229], v[212:213]\n" \
  "v_pk_mul_f32 v[230:231], v[230:231], v[214:215]\n" \
  "v_pk_mul_f32 v[232:233], v[232:233], v[216:217]\n" \
  "v_pk_mul_f32 v[234:235], v[234:235], v[218:219]\n" \
  "v_pk_mul_f32 v[236:237], v[236:237], v[220:221]\n" \
  "v_pk_mul_f32 v[238:239], v[238:239], v[222:223]\n" \
  "v_cvt_pk_bf16_f32 v240, v224, v225\n" \
  "v_cvt_pk_bf16_f32 v241, v226, v227\n" \
  "v_cvt_pk_bf16_f32 v242, v228, v229\n" \
  "v_cvt_pk_bf16_f32 v243, v230, v231\n" \
  "v_cvt_pk_bf16_f32 v244, v232, v233\n" \
  "v_cvt_pk_bf16_f32 v245, v234, v235\n" \
  "v_cvt_pk_bf16_f32 v246, v236, v237\n" \
  "v_cvt_pk_bf16_f32 v247, v238, v239\n" \
  "s_nop 1\n" \
  "v_mfma_f32_32x32x16_bf16 %3, v[240:243], %12, %3\n" \
  "v_mfma_f32_32x32x16_bf16 %3, v[244:247], %13, %3\n" \
  "s_branch .Lblke_%=\n" \
  ".Lblk3_%=:\n" \
  "s_cmp_gt_i32 %22, 16\n" \
  "s_cbranch_scc0 .Lblk2_%=\n" \
  "v_mov_b32 v246, 0\n" \
  "v_mov_b32 v247, 0\n" \
  "v_mfma_f32_32x32x16_bf16 v[192:207], %14, %17, 0\n" \
  "v_mfma_f32_32x32x16_bf16 v[192:207], %15, %4, v[192:207]\n" \
  "v_mfma_f32_32x32x16_bf16 v[192:207], %16, %5, v[192:207]\n" \
  "s_nop 11\n" \
  "v_exp_f32 v224, v192\n" \
  "v_exp_f32 v225, v193\n" \
  "v_exp_f32 v226, v194\n" \
  "v_exp_f32 v227, v195\n" \
  "v_exp_f32 v228, v196\n" \
  "v_exp_f32 v229, v197\n" \
  "v_exp_f32 v230, v198\n" \
  "v_exp_f32 v231, v199\n" \
  "v_mfma_f32_32x32x16_bf16 v[208:223], %14, %18, 0\n" \
  "v_exp_f32 v232, v200\n" \
  "v_exp_f32 v233, v201\n" \
  "v_exp_f32 v234, v202\n" \
  "v_exp_f32 v235, v203\n" \
  "v_pk_fma_f32 v[224:225], v[224:225], %21, %21\n" \
  "v_pk_fma_f32 v[226:227], v[226:227], %21, %21\n" \
  "v_pk_fma_f32 v[228:229], v[228:229], %21, %21\n" \
  "v_pk_fma_f32 v[230:231], v[230:231], %21, %21\n" \
  "v_pk_fma_f32 v[232:233], v[232:233], %21, %21\n" \
  "v_pk_fma_f32 v[234:235], v[234:235], %21, %21\n" \
  "v_mfma_f32_32x32x16_bf16 v[208:223], %15, %6, v[208:223]\n" \
  "v_rcp_f32 v224, v224\n" \
  "v_rcp_f32 v225, v225\n" \
  "v_rcp_f32 v226, v226\n" \
  "v_mfma_f32_32x32x16_bf16 v[208:223], %16, %7, v[208:223]\n" \
  "v_rcp_f32 v227, v227\n" \
  "v_rcp_f32 v228, v228\n" \
  "v_rcp_f32 v229, v229\n" \
  "v_rcp_f32 v230, v230\n" \
  "v_rcp_f32 v231, v231\n" \
  "v_rcp_f32 v232, v232\n" \
  "v_rcp_f32 v233, v233\n" \
  "v_rcp_f32 v234, v234\n" \
  "v_rcp_f32 v235, v235\n" \
  "v_pk_mul_f32 v[224:225], v[224:225], v[192:193]\n" \
  "v_pk_mul_f32 v[226:227], v[226:227], v[194:195]\n" \
  "v_pk_mul_f32 v[228:229], v[228:229], v[196:197]\n" \
  "v_pk_mul_f32 v[230:231], v[230:231], v[198:199]\n" \
  "v_pk_mul_f32 v[232:233], v[232:233], v[200:201]\n" \
  "v_pk_mul_f32 v[234:235], v[234:235], v[202:203]\n" \
  "v_cvt_pk_bf16_f32 v240, v224, v225\n" \
  "v_cvt_pk_bf16_f32 v241, v226, v227\n" \
  "v_cvt_pk_bf16_f32 v242, v228, v229\n" \
  "v_cvt_pk_bf16_f32 v243, v230, v231\n" \
  "v_cvt_pk_bf16_f32 v244, v232, v233\n" \
  "v_cvt_pk_bf16_f32 v245, v234, v235\n" \
  "v_exp_f32 v224, v208\n" \
  "v_exp_f32 v225, v209\n" \
  "v_mfma_f32_32x32x16_bf16 %0, v[240:243], %12, %0\n" \
  "v_exp_f32 v226, v210\n" \
  "v_exp_f32 v227, v211\n" \
  "v_exp_f32 v228, v212\n" \
  "v_mfma_f32_32x32x16_bf16 %0, v[244:247], %13, %0\n" \
  "v_exp_f32 v229, v213\n" \
  "v_exp_f32 v230, v214\n" \
  "v_exp_f32 v231, v215\n" \
  "v_mfma_f32_32x32x16_bf16 v[192:207], %14, %19, 0\n" \
  "v_exp_f32 v232, v216\n" \
  "v_exp_f32 v233, v217\n" \
  "v_exp_f32 v234, v218\n" \
  "v_exp_f32 v235, v219\n" \
  "v_pk_fma_f32 v[224:225], v[224:225], %21, %21\n" \
  "v_pk_fma_f32 v[226:227], v[226:227], %21, %21\n" \
  "v_pk_fma_f32 v[228:229], v[228:229], %21, %21\n" \
  "v_pk_fma_f32 v[230:231], v[230:231], %21, %21\n" \
  "v_pk_fma_f32 v[232:233], v[232:233], %21, %21\n" \
  "v_pk_fma_f32 v[234:235], v[234:235], %21, %21\n" \
  "v_mfma_f32_32x32x16_bf16 v[192:207], %15, %8, v[192:207]\n" \
  "v_rcp_f32 v224, v224\n" \
  "v_rcp_f32 v225, v225\n" \
  "v_rcp_f32 v226, v226\n" \
  "v_mfma_f32_32x32x16_bf16 v[192:207], %16, %9, v[192:207]\n" \
  "v_rcp_f32 v227, v227\n" \
  "v_rcp_f32 v228, v228\n" \
  "v_rcp_f32 v229, v229\n" \
  "v_rcp_f32 v230, v230\n" \
  "v_rcp_f32 v231, v231\n" \
  "v_rcp_f32 v232, v232\n" \
  "v_rcp_f32 v233, v233\n" \
  "v_rcp_f32 v234, v234\n" \
  "v_rcp_f32 v235, v235\n" \
  "v_pk_mul_f32 v[224:225], v[224:225], v[208:209]\n" \
  "v_pk_mul_f32 v[226:227], v[226:227], v[210:211]\n" \
  "v_pk_mul_f32 v[228:229], v[228:229], v[212:213]\n" \
  "v_pk_mul_f32 v[230:231], v[230:231], v[214:215]\n" \
  "v_pk_mul_f32 v[232:233], v[232:233], v[216:217]\n" \
  "v_pk_mul_f32 v[234:235], v[234:235], v[218:219]\n" \
  "v_cvt_pk_bf16_f32 v240, v224, v225\n" \
  "v_cvt_pk_bf16_f32 v241, v226, v227\n" \
  "v_cvt_pk_bf16_f32 v242, v228, v229\n" \
  "v_cvt_pk_bf16_f32 v243, v230, v231\n" \
  "v_cvt_pk_bf16_f32 v244, v232, v233\n" \
  "v_cvt_pk_bf16_f32 v245, v234, v235\n" \
  "v_exp_f32 v224, v192\n" \
  "v_exp_f32 v225, v193\n" \
  "v_mfma_f32_32x32x16_bf16 %1, v[240:243], %12, %1\n" \
  "v_exp_f32 v226, v194\n" \
  "v_exp_f32 v227, v195\n" \
  "v_exp_f32 v228, v196\n" \
  "v_mfma_f32_32x32x16_bf16 %1, v[244:247], %13, %1\n" \
  "v_exp_f32 v229, v197\n" \
  "v_exp_f32 v230, v198\n" \
  "v_exp_f32 v231, v199\n" \
  "v_mfma_f32_32x32x16_bf16 v[208:223], %14, %20, 0\n" \
  "v_exp_f32 v232, v200\n" \
  "v_exp_f32 v233, v201\n" \
  "v_exp_f32 v234, v202\n" \
  "v_exp_f32 v235, v203\n" \
  "v_pk_fma_f32 v[224:225], v[224:225], %21, %21\n" \
  "v_pk_fma_f32 v[226:227], v[226:227], %21, %21\n" \
  "v_pk_fma_f32 v[228:229], v[228:229], %21, %21\n" \
  "v_pk_fma_f32 v[230:231], v[230:231], %21, %21\n" \
  "v_pk_fma_f32 v[232:233], v[232:233], %21, %21\n" \
  "v_pk_fma_f32 v[234:235], v[234:235], %21, %21\n" \
  "v_mfma_f32_32x32x16_bf16 v[208:223], %15, %10, v[208:223]\n" \
  "v_rcp_f32 v224, v224\n" \
  "v_rcp_f32 v225, v225\n" \
  "v_rcp_f32 v226, v226\n" \
  "v_mfma_f32_32x32x16_bf16 v[208:223], %16, %11, v[208:223]\n" \
  "v_rcp_f32 v227, v227\n" \
  "v_rcp_f32 v228, v228\n" \
  "v_rcp_f32 v229, v229\n" \
  "v_rcp_f32 v230, v230\n" \
  "v_rcp_f32 v231, v231\n" \
  "v_rcp_f32 v232, v232\n" \
  "v_rcp_f32 v233, v233\n" \
  "v_rcp_f32 v234, v234\n" \
  "v_rcp_f32 v235, v235\n" \
  "v_pk_mul_f32 v[224:225], v[224:225], v[192:193]\n" \
  "v_pk_mul_f32 v[226:227], v[226:227], v[194:195]\n" \
  "v_pk_mul_f32 v[228:229], v[228:229], v[196:197]\n" \
  "v_pk_mul_f32 v[230:231], v[230:231], v[198:199]\n" \
  "v_pk_mul_f32 v[232:233], v[232:233], v[200:201]\n" \
  "v_pk_mul_f32 v[234:235], v[234:235], v[202:203]\n" \
  "v_cvt_pk_bf16_f32 v240, v224, v225\n" \
  "v_cvt_pk_bf16_f32 v241, v226, v227\n" \
  "v_cvt_pk_bf16_f32 v242, v228, v229\n" \
  "v_cvt_pk_bf16_f32 v243, v230, v231\n" \
  "v_cvt_pk_bf16_f32 v244, v232, v233\n" \
  "v_cvt_pk_bf16_f32 v245, v234, v235\n" \
  "v_exp_f32 v224, v208\n" \
  "v_exp_f32 v225, v209\n" \
  "v_mfma_f32_32x32x16_bf16 %2, v[240:243], %12, %2\n" \
  "v_exp_f32 v226, v210\n" \
  "v_exp_f32 v227, v211\n" \
  "v_exp_f32 v228, v212\n" \
  "v_mfma_f32_32x32x16_bf16 %2, v[244:247], %13, %2\n" \
  "v_exp_f32 v229, v213\n" \
  "v_exp_f32 v230, v214\n" \
  "v_exp_f32 v231, v215\n" \
  "v_exp_f32 v232, v216\n" \
  "v_exp_f32 v233, v217\n" \
  "v_exp_f32 v234, v218\n" \
  "v_exp_f32 v235, v219\n" \
  "v_pk_fma_f32 v[224:225], v[224:225], %21, %21\n" \
  "v_pk_fma_f32 v[226:227], v[226:227], %21, %21\n" \
  "v_pk_fma_f32 v[228:229], v[228:229], %21, %21\n" \
  "v_pk_fma_f32 v[230:231], v[230:231], %21, %21\n" \
  "v_pk_fma_f32 v[232:233], v[232:233], %21, %21\n" \
  "v_pk_fma_f32 v[234:235], v[234:235], %21, %21\n" \
  "v_rcp_f32 v224, v224\n" \
  "v_rcp_f32 v225, v225\n" \
  "v_rcp_f32 v226, v226\n" \
  "v_rcp_f32 v227, v227\n" \
  "v_rcp_f32 v228, v228\n" \
  "v_rcp_f32 v229, v229\n" \
  "v_rcp_f32 v230, v230\n" \
  "v_rcp_f32 v231, v231\n" \
  "v_rcp_f32 v232, v232\n" \
  "v_rcp_f32 v233, v233\n" \
  "v_rcp_f32 v234, v234\n" \
  "v_rcp_f32 v235, v235\n" \
  "v_pk_mul_f32 v[224:225], v[224:225], v[208:209]\n" \
  "v_pk_mul_f32 v[226:227], v[226:227], v[210:211]\n" \
  "v_pk_mul_f32 v[228:229], v[228:229], v[212:213]\n" \
  "v_pk_mul_f32 v[230:231], v[230:231], v[214:215]\n" \
  "v_pk_mul_f32 v[232:233], v[232:233], v[216:217]\n" \
  "v_pk_mul_f32 v[234:235], v[234:235], v[218:219]\n" \
  "v_cvt_pk_bf16_f32 v240, v224, v225\n" \
  "v_cvt_pk_bf16_f32 v241, v226, v227\n" \
  "v_cvt_pk_bf16_f32 v242, v228, v229\n" \
  "v_cvt_pk_bf16_f32 v243, v230, v231\n" \
  "v_cvt_pk_bf16_f32 v244, v232, v233\n" \
  "v_cvt_pk_bf16_f32 v245, v234, v235\n" \
  "s_nop 1\n" \
  "v_mfma_f32_32x32x16_bf16 %3, v[240:243], %12, %3\n" \
  "v_mfma_f32_32x32x16_bf16 %3, v[244:247], %13, %3\n" \
  "s_branch .Lblke_%=\n" \
  ".Lblk2_%=:\n" \
  "s_cmp_gt_i32 %22, 8\n" \
  "s_cbranch_scc0 .Lblk1_%=\n" \
  "v_mfma_f32_32x32x16_bf16 v[192:207], %14, %17, 0\n" \
  "v_mfma_f32_32x32x16_bf16 v[192:207], %15, %4, v[192:207]\n" \
  "s_nop 11\n" \
  "v_exp_f32 v224, v192\n" \
  "v_exp_f32 v225, v193\n" \
  "v_exp_f32 v226, v194\n" \
  "v_exp_f32 v227, v195\n" \
  "v_mfma_f32_32x32x16_bf16 v[208:223], %14, %18, 0\n" \
  "v_exp_f32 v228, v196\n" \
  "v_exp_f32 v229, v197\n" \
  "v_exp_f32 v230, v198\n" \
  "v_exp_f32 v231, v199\n" \
  "v_pk_fma_f32 v[224:225], v[224:225], %21, %21\n" \
  "v_pk_fma_f32 v[226:227], v[226:227], %21, %21\n" \
  "v_pk_fma_f32 v[228:229], v[228:229], %21, %21\n" \
  "v_pk_fma_f32 v[230:231], v[230:231], %21, %21\n" \
  "v_mfma_f32_32x32x16_bf16 v[208:223], %15, %6, v[208:223]\n" \
  "v_rcp_f32 v224, v224\n" \
  "v_rcp_f32 v225, v225\n" \
  "v_rcp_f32 v226, v226\n" \
  "v_rcp_f32 v227, v227\n" \
  "v_rcp_f32 v228, v228\n" \
  "v_rcp_f32 v229, v229\n" \
  "v_rcp_f32 v230, v230\n" \
  "v_rcp_f32 v231, v231\n" \
  "v_pk_mul_f32 v[224:225], v[224:225], v[192:193]\n" \
  "v_pk_mul_f32 v[226:227], v[226:227], v[194:195]\n" \
  "v_pk_mul_f32 v[228:229], v[228:229], v[196:197]\n" \
  "v_pk_mul_f32 v[230:231], v[230:231], v[198:199]\n" \
  "v_cvt_pk_bf16_f32 v240, v224, v225\n" \
  "v_cvt_pk_bf16_f32 v241, v226, v227\n" \
  "v_cvt_pk_bf16_f32 v242, v228, v229\n" \
  "v_cvt_pk_bf16_f32 v243, v230, v231\n" \
  "v_exp_f32 v224, v208\n" \
  "v_exp_f32 v225, v209\n" \
  "v_mfma_f32_32x32x16_bf16 %0, v[240:243], %12, %0\n" \
  "v_exp_f32 v226, v210\n" \
  "v_exp_f32 v227, v211\n" \
  "v_mfma_f32_32x32x16_bf16 v[192:207], %14, %19, 0\n" \
  "v_exp_f32 v228, v212\n" \
  "v_exp_f32 v229, v213\n" \
  "v_exp_f32 v230, v214\n" \
  "v_exp_f32 v231, v215\n" \
  "v_pk_fma_f32 v[224:225], v[224:225], %21, %21\n" \
  "v_pk_fma_f32 v[226:227], v[226:227], %21, %21\n" \
  "v_pk_fma_f32 v[228:229], v[228:229], %21, %21\n" \
  "v_pk_fma_f32 v[230:231], v[230:231], %21, %21\n" \
  "v_mfma_f32_32x32x16_bf16 v[192:207], %15, %8, v[192:207]\n" \
  "v_rcp_f32 v224, v224\n" \
  "v_rcp_f32 v225, v225\n" \
  "v_rcp_f32 v226, v226\n" \
  "v_rcp_f32 v227, v227\n" \
  "v_rcp_f32 v228, v228\n" \
  "v_rcp_f32 v229, v229\n" \
  "v_rcp_f32 v230, v230\n" \
  "v_rcp_f32 v231, v231\n" \
  "v_pk_mul_f32 v[224:225], v[224:225], v[208:209]\n" \
  "v_pk_mul_f32 v[226:227], v[226:227], v[210:211]\n" \
  "v_pk_mul_f32 v[228:229], v[228:229], v[212:213]\n" \
  "v_pk_mul_f32 v[230:231], v[230:231], v[214:215]\n" \
  "v_cvt_pk_bf16_f32 v240, v224, v225\n" \
  "v_cvt_pk_bf16_f32 v241, v226, v227\n" \
  "v_cvt_pk_bf16_f32 v242, v228, v229\n" \
  "v_cvt_pk_bf16_f32 v243, v230, v231\n" \
  "v_exp_f32 v224, v192\n" \
  "v_exp_f32 v225, v193\n" \
  "v_mfma_f32_32x32x16_bf16 %1, v[240:243], %12, %1\n" \
  "v_exp_f32 v226, v194\n" \
  "v_exp_f32 v227, v195\n" \
  "v_mfma_f32_32x32x16_bf16 v[208:223], %14, %20, 0\n" \
  "v_exp_f32 v228, v196\n" \
  "v_exp_f32 v229, v197\n" \
  "v_exp_f32 v230, v198\n" \
  "v_exp_f32 v231, v199\n" \
  "v_pk_fma_f32 v[224:225], v[224:225], %21, %21\n" \
  "v_pk_fma_f32 v[226:227], v[226:227], %21, %21\n" \
  "v_pk_fma_f32 v[228:229], v[228:229], %21, %21\n" \
  "v_pk_fma_f32 v[230:231], v[230:231], %21, %21\n" \
  "v_mfma_f32_32x32x16_bf16 v[208:223], %15, %10, v[208:223]\n" \
  "v_rcp_f32 v224, v224\n" \
  "v_rcp_f32 v225, v225\n" \
  "v_rcp_f32 v226, v226\n" \
  "v_rcp_f32 v227, v227\n" \
  "v_rcp_f32 v228, v228\n" \
  "v_rcp_f32 v229, v229\n" \
  "v_rcp_f32 v230, v230\n" \
  "v_rcp_f32 v231, v231\n" \
  "v_pk_mul_f32 v[224:225], v[224:225], v[192:193]\n" \
  "v_pk_mul_f32 v[226:227], v[226:227], v[194:195]\n" \
  "v_pk_mul_f32 v[228:229], v[228:229], v[196:197]\n" \
  "v_pk_mul_f32 v[230:231], v[230:231], v[198:199]\n" \
  "v_cvt_pk_bf16_f32 v240, v224, v225\n" \
  "v_cvt_pk_bf16_f32 v241, v226, v227\n" \
  "v_cvt_pk_bf16_f32 v242, v228, v229\n" \
  "v_cvt_pk_bf16_f32 v243, v230, v231\n" \
  "v_exp_f32 v224, v208\n" \
  "v_exp_f32 v225, v209\n" \
  "v_mfma_f32_32x32x16_bf16 %2, v[240:243], %12, %2\n" \
  "v_exp_f32 v226, v210\n" \
  "v_exp_f32 v227, v211\n" \
  "v_exp_f32 v228, v212\n" \
  "v_exp_f32 v229, v213\n" \
  "v_exp_f32 v230, v214\n" \
  "v_exp_f32 v231, v215\n" \
  "v_pk_fma_f32 v[224:225], v[224:225], %21, %21\n" \
  "v_pk_fma_f32 v[226:227], v[226:227], %21, %21\n" \
  "v_pk_fma_f32 v[228:229], v[228:229], %21, %21\n" \
  "v_pk_fma_f32 v[230:231], v[230:231], %21, %21\n" \
  "v_rcp_f32 v224, v224\n" \
  "v_rcp_f32 v225, v225\n" \
  "v_rcp_f32 v226, v226\n" \
  "v_rcp_f32 v227, v227\n" \
  "v_rcp_f32 v228, v228\n" \
  "v_rcp_f32 v229, v229\n" \
  "v_rcp_f32 v230, v230\n" \
  "v_rcp_f32 v231, v231\n" \
  "v_pk_mul_f32 v[224:225], v[224:225], v[208:209]\n" \
  "v_pk_mul_f32 v[226:227], v[226:227], v[210:211]\n" \
  "v_pk_mul_f32 v[228:229], v[228:229], v[212:213]\n" \
  "v_pk_mul_f32 v[230:231], v[230:231], v[214:215]\n" \
  "v_cvt_pk_bf16_f32 v240, v224, v225\n" \
  "v_cvt_pk_bf16_f32 v241, v226, v227\n" \
  "v_cvt_pk_bf16_f32 v242, v228, v229\n" \
  "v_cvt_pk_bf16_f32 v243, v230, v231\n" \
  "s_nop 1\n" \
  "v_mfma_f32_32x32x16_bf16 %3, v[240:243], %12, %3\n" \
  "s_branch .Lblke_%=\n" \
  ".Lblk1_%=:\n" \
  "v_mov_b32 v242, 0\n" \
  "v_mov_b32 v243, 0\n" \
  "v_mfma_f32_32x32x16_bf16 v[192:207], %14, %17, 0\n" \
  "v_mfma_f32_32x32x16_bf16 v[192:207], %15, %4, v[192:207]\n" \
  "s_nop 11\n" \
  "v_exp_f32 v224, v192\n" \
  "v_exp_f32 v225, v193\n" \
  "v_exp_f32 v226, v194\n" \
  "v_exp_f32 v227, v195\n" \
  "v_pk_fma_f32 v[224:225], v[224:225], %21, %21\n" \
  "v_pk_fma_f32 v[226:227], v[226:227], %21, %21\n" \
  "v_mfma_f32_32x32x16_bf16 v[208:223], %14, %18, 0\n" \
  "v_rcp_f32 v224, v224\n" \
  "v_rcp_f32 v225, v225\n" \
  "v_rcp_f32 v226, v226\n" \
  "v_rcp_f32 v227, v227\n" \
  "v_pk_mul_f32 v[224:225], v[224:225], v[192:193]\n" \
  "v_pk_mul_f32 v[226:227], v[226:227], v[194:195]\n" \
  "v_mfma_f32_32x32x16_bf16 v[208:223], %15, %6, v[208:223]\n" \
  "v_cvt_pk_bf16_f32 v240, v224, v225\n" \
  "v_cvt_pk_bf16_f32 v241, v226, v227\n" \
  "s_nop 9\n" \
  "v_exp_f32 v224, v208\n" \
  "v_exp_f32 v225, v209\n" \
  "v_mfma_f32_32x32x16_bf16 %0, v[240:243], %12, %0\n" \
  "v_exp_f32 v226, v210\n" \
  "v_exp_f32 v227, v211\n" \
  "v_pk_fma_f32 v[224:225], v[224:225], %21, %21\n" \
  "v_pk_fma_f32 v[226:227], v[226:227], %21, %21\n" \
  "v_mfma_f32_32x32x16_bf16 v[192:207], %14, %19, 0\n" \
  "v_rcp_f32 v224, v224\n" \
  "v_rcp_f32 v225, v225\n" \
  "v_rcp_f32 v226, v226\n" \
  "v_rcp_f32 v227, v227\n" \
  "v_pk_mul_f32 v[224:225], v[224:225], v[208:209]\n" \
  "v_pk_mul_f32 v[226:227], v[226:227], v[210:211]\n" \
  "v_mfma_f32_32x32x16_bf16 v[192:207], %15, %8, v[192:207]\n" \
  "v_cvt_pk_bf16_f32 v240, v224, v225\n" \
  "v_cvt_pk_bf16_f32 v241, v226, v227\n" \
  "s_nop 9\n" \
  "v_exp_f32 v224, v192\n" \
  "v_exp_f32 v225, v193\n" \
  "v_mfma_f32_32x32x16_bf16 %1, v[240:243], %12, %1\n" \
  "v_exp_f32 v226, v194\n" \
  "v_exp_f32 v227, v195\n" \
  "v_pk_fma_f32 v[224:225], v[224:225], %21, %21\n" \
  "v_pk_fma_f32 v[226:227], v[226:227], %21, %21\n" \
  "v_mfma_f32_32x32x16_bf16 v[208:223], %14, %20, 0\n" \
  "v_rcp_f32 v224, v224\n" \
  "v_rcp_f32 v225, v225\n" \
  "v_rcp_f32 v226, v226\n" \
  "v_rcp_f32 v227, v227\n" \
  "v_pk_mul_f32 v[224:225], v[224:225], v[192:193]\n" \
  "v_pk_mul_f32 v[226:227], v[226:227], v[194:195]\n" \
  "v_mfma_f32_32x32x16_bf16 v[208:223], %15, %10, v[208:223]\n" \
  "v_cvt_pk_bf16_f32 v240, v224, v225\n" \
  "v_cvt_pk_bf16_f32 v241, v226, v227\n" \
  "s_nop 9\n" \
  "v_exp_f32 v224, v208\n" \
  "v_exp_f32 v225, v209\n" \
  "v_mfma_f32_32x32x16_bf16 %2, v[240:243], %12, %2\n" \
  "v_exp_f32 v226, v210\n" \
  "v_exp_f32 v227, v211\n" \
  "v_pk_fma_f32 v[224:225], v[224:225], %21, %21\n" \
  "v_pk_fma_f32 v[226:227], v[226:227], %21, %21\n" \
  "v_rcp_f32 v224, v224\n" \
  "v_rcp_f32 v225, v225\n" \
  "v_rcp_f32 v226, v226\n" \
  "v_rcp_f32 v227, v227\n" \
  "v_pk_mul_f32 v[224:225], v[224:225], v[208:209]\n" \
  "v_pk_mul_f32 v[226:227], v[226:227], v[210:211]\n" \
  "v_cvt_pk_bf16_f32 v240, v224, v225\n" \
  "v_cvt_pk_bf16_f32 v241, v226, v227\n" \
  "s_nop 1\n" \
  "v_mfma_f32_32x32x16_bf16 %3, v[240:243], %12, %3\n" \
  ".Lblke_%=:\n"
#define BLK_CLOBBERS "v192", "v193", "v194", "v195", "v196", "v197", "v198", "v199", "v200", "v201", "v202", "v203", "v204", "v205", "v206", "v207", "v208", "v209", "v210", "v211", "v212", "v213", "v214", "v215", "v216", "v217", "v218", "v219", "v220", "v221", "v222", "v223", "v224", "v225", "v226", "v227", "v228", "v229", "v230", "v231", "v232", "v233", "v234", "v235", "v236", "v237", "v238", "v239", "v240", "v241", "v242", "v243", "v244", "v245", "v246", "v247", "scc"
struct Frags { bf16x8 b[4][2]; bf16x8 s[2]; bf16x8 e; };
DI void block_compute(const Frags& f, const bf16x8 (&id)[2], const bf16x8 (&eb)[4], f32x16 (&acc)[4], f32x2 cc, int rem) {
  asm volatile(BLK_ASM
               : "+v"(acc[0]), "+v"(acc[1]), "+v"(acc[2]), "+v"(acc[3])
               : "v"(f.b[0][0]), "v"(f.b[0][1]), "v"(f.b[1][0]), "v"(f.b[1][1]), "v"(f.b[2][0]), "v"(f.b[2][1]), "v"(f.b[3][0]),
                 "v"(f.b[3][1]), "v"(f.s[0]), "v"(f.s[1]), "v"(f.e), "v"(id[0]), "v"(id[1]), "v"(eb[0]), "v"(eb[1]), "v"(eb[2]),
                 "v"(eb[3]), "s"(cc), "s"(rem)
               : BLK_CLOBBERS);
}

DI u32 act1(float y) {
  const float r = __builtin_amdgcn_rcpf(__builtin_fmaf(__builtin_amdgcn_exp2f(y), CL2, CL2));
  return pk_bf16(y * r, 0.f);
}

__global__ __launch_bounds__(512, 2) void k_main(const u16* __restrict__ h, const u16* __restrict__ pos, const u16* __restrict__ ea,
                                                 const int* __restrict__ srcg, const int* __restrict__ dstg,
                                                 const u16* __restrict__ we, const u16* __restrict__ wx,
                                                 const char* __restrict__ ws, u16* __restrict__ out
) {
  __shared__ __attribute__((aligned(16))) char lds[L_TOTAL];
  const int tid = threadIdx.x, lane = tid & 63;
  const int w = __builtin_amdgcn_readfirstlane(tid >> 6);
  const int b = blockIdx.x;
  const int n0 = node0_of(b), nn = node0_of(b + 1) - n0;
  const u32 ldsbase = (u32)(size_t)(LDSP char*)lds;
  const char* bp = ws + WS_BP;
  STAMP_DECL
  STAMP(0);

  const int l15 = lane & 15, g4 = lane >> 4;
  const int hr0 = tid >> 4, hr1 = 32 + ((tid >> 4) & 15);
  u32x4 q0, ht0, ht1, w1f[4], whf[4], wh2[4];
  u32 tb0, tb1, tb2;
  gld16(q0, ws + WS_SLOTS, (u32)(b * (NPROD * 8) + tid) * 16u);
  gld16(ht0, h, (u32)((n0 + hr0) * 256 + l15 * 16));
  gld16(ht1, h, (u32)((n0 + min(hr1, nn - 1)) * 256 + l15 * 16));
  gld2p(tb0, tid < 128 ? we + 256 * 128 + tid : pos + n0 * 3 + min(tid - 128, 3 * nn - 1));
  gld2(tb1, we + 257 * 128, (u32)(tid & 127) * 2u);
  gld2(tb2, wx, (u32)(tid & 127) * 2u);
  {
    const u32 v1 = (u32)(w * 4096 + lane * 16), v2 = (u32)(w * 8192 + lane * 16);
    gld16o<0>(w1f[0], ws + WS_W1F, v1); gld16o<1024>(w1f[1], ws + WS_W1F, v1);
    gld16o<2048>(w1f[2], ws + WS_W1F, v1); gld16o<3072>(w1f[3], ws + WS_W1F, v1);
    gld16o<0>(whf[0], ws + WS_WHF, v2); gld16o<1024>(whf[1], ws + WS_WHF, v2);
    gld16o<2048>(whf[2], ws + WS_WHF, v2); gld16o<3072>(whf[3], ws + WS_WHF, v2);
    gld16o<0>(wh2[0], ws + WS_WHF + 4096, v2); gld16o<1024>(wh2[1], ws + WS_WHF + 4096, v2);
    gld16o<2048>(wh2[2], ws + WS_WHF + 4096, v2); gld16o<3072>(wh2[3], ws + WS_WHF + 4096, v2);
  }
  if (tid < 64) lds_st<u32>(lds, L_CNT + tid * 4, 0u);
  wg_barrier();
  STAMP(1);

  asm volatile("s_waitcnt vmcnt(17)" : "+v"(q0) :: "memory");
  u32 rk[4];
  const int ch = tid & 7;
  const u32 c0 = (u32)__builtin_amdgcn_ds_swizzle((int)q0[0], 0x18);
  u32* cnt = (u32*)(lds + L_CNT);
  if (c0 > CAP) cnt[63] = 1;
#pragma unroll
  for (int j = 0; j < 4; ++j) {
    const bool valid = (u32)(ch * 4 + j - 1) < min(c0, (u32)CAP);
    rk[j] = valid ? atomicAdd(&cnt[(q0[j] >> 14) & 63], 1u) : 0xffffffffu;
  }
  wg_barrier();
  STAMP(2);

  const u32 cl = lane < nn ? cnt[lane] : 0u;
  const bool ovf = __builtin_amdgcn_readfirstlane(cnt[63]) != 0;
  u32 incl = cl;
  incl += __builtin_amdgcn_update_dpp(0u, incl, 0x111, 0xf, 0xf, false);
  incl += __builtin_amdgcn_update_dpp(0u, incl, 0x112, 0xf, 0xf, false);
  incl += __builtin_amdgcn_update_dpp(0u, incl, 0x114, 0xf, 0xf, false);
  incl += __builtin_amdgcn_update_dpp(0u, incl, 0x118, 0xf, 0xf, false);
  incl += __builtin_amdgcn_update_dpp(0u, incl, 0x142, 0xa, 0xf, false);
  incl += __builtin_amdgcn_update_dpp(0u, incl, 0x143, 0xc, 0xf, false);
  const u32 total = (u32)__builtin_amdgcn_readlane((int)incl, 63);
  const u32 excl = incl - cl;
  const bool slow = ovf || total > LCAP;
#pragma unroll
  for (int j = 0; j < 4; ++j) {
    const u32 nl = (q0[j] >> 14) & 63;
    const u32 st = (u32)__builtin_amdgcn_ds_bpermute((int)(nl * 4), (int)excl);
    if (rk[j] != 0xffffffffu && !slow) {
      const u32 pp = st + rk[j];
      const u32 e = (u32)(tid >> 3) * CHUNK + (q0[j] >> 20);
      lds_st<u32>(lds, L_LIST + pp * 4, (q0[j] & 0x3fffu) | (e << 14));
      lds_st<unsigned char>(lds, L_NL + pp, (unsigned char)nl);
    }
  }
  int rid = (int)((float)(2 * excl + cl) * (4.0f * __builtin_amdgcn_rcpf((float)max(total, 1u))));
  if (lane >= nn) rid = 99;
  const int myr = 2 * (w & 3) + (w >> 2);
  int g_n = 0, g_end = 0;
  {
    int cur = 0;
#pragma unroll
    for (int k = 0; k < 8; ++k) {
      const int ck = k == 7 ? nn : __popcll(__ballot(rid <= k));
      const int nxt = max(min(ck, cur + 8), nn - 8 * (7 - k));
      if (k == myr) { g_n = cur; g_end = nxt; }
      cur = nxt;
    }
  }
  const int g_cnt = g_end - g_n;
  const int eb = __builtin_amdgcn_readlane((int)excl, g_n);
  const int ee = slow ? eb : __builtin_amdgcn_readlane((int)excl, g_end);
  asm volatile("s_waitcnt vmcnt(12)" : "+v"(ht0), "+v"(ht1), "+v"(tb0), "+v"(tb1), "+v"(tb2) :: "memory");
  if (tid < 128) {
    const float wd = bf2f(tb0) * CL2, wa = bf2f(tb1) * CL2;
    const u32 hi = pk_bf16(wd, wa);
    const u32 lo = pk_bf16(wd - lo_f(hi), wa - hi_f(hi));
    lds_st<u16>(lds, L_AIMG + 48 * AP + tid * 2, (u16)hi);
    lds_st<u16>(lds, L_AIMG + 49 * AP + tid * 2, (u16)(hi >> 16));
    lds_st<u16>(lds, L_AIMG + 50 * AP + tid * 2, (u16)lo);
    lds_st<u16>(lds, L_AIMG + 51 * AP + tid * 2, (u16)(lo >> 16));
    lds_st<u16>(lds, L_AIMG + 52 * AP + tid * 2, (u16)0);
    lds_st<float>(lds, L_WX + tid * 4, bf2f(tb2));
  } else if (tid < 128 + 3 * nn) {
    const int t = tid - 128, i = t / 3, c = t - 3 * i;
    lds_st<float>(lds, L_POS + i * 16 + c * 4, bf2f(tb0));
  }
  lds_st<u32x4>(lds, L_HT + hr0 * 256 + ((l15 ^ (hr0 & 15)) * 16), ht0);
  if (tid < 256) lds_st<u32x4>(lds, L_HT + hr1 * 256 + ((l15 ^ (hr1 & 15)) * 16), ht1);
  wg_barrier();
  STAMP(3);

  const int r = lane & 31, hh = lane >> 5;
  const int rowbuf = L_ROWS + w * 8192;
  const int sbase = L_S + w * 2560;
  const int tq = l15 >> 2, tp = l15 & 3;
  const u32 dc = (u32)(((lane & 7) ^ ((g4 & 1) << 2)) * 16);
  const int l8 = lane >> 3;

  auto issue_rows = [&](int base, int last, u32 mask) {
    u32 v[4];
#pragma unroll
    for (int pc = 0; pc < 4; ++pc) {
      const u32 ent = lds_ld<u32>(lds, L_LIST + max(min(base + 8 * pc + l8, last), 0) * 4);
      v[pc] = (ent & mask) * 256u + dc;
    }
    dma_rows(bp, v[0], v[1], v[2], v[3], ldsbase + rowbuf);
  };

  u32 ent[2], nlv[2], eav[2];
  u32x2 ps[2];
#pragma unroll
  for (int rd = 0; rd < 2; ++rd) {
    const int i = eb + lane + 64 * rd;
    const bool ok = i < ee;
    ent[rd] = ok ? lds_ld<u32>(lds, L_LIST + i * 4) : 0u;
    nlv[rd] = ok ? lds_ld<unsigned char>(lds, L_NL + i) : 0u;
  }
#pragma unroll
  for (int rd = 0; rd < 2; ++rd) {
    gld8(ps[rd], pos, ((ent[rd] & 0x3fffu) * 6u) & ~3u);
    gld2(eav[rd], ea, (ent[rd] >> 14) * 2u);
  }
  issue_rows(eb, ee - 1, ee > eb ? 0x3fffu : 0u);
  asm volatile("s_waitcnt vmcnt(16)" : "+v"(w1f[0]), "+v"(w1f[1]), "+v"(w1f[2]), "+v"(w1f[3]), "+v"(whf[0]), "+v"(whf[1]),
                                       "+v"(whf[2]), "+v"(whf[3]) :: "memory");

  f32x4 z[3];
#pragma unroll
  for (int nt = 0; nt < 3; ++nt) {
    f32x4 a = {0.f, 0.f, 0.f, 0.f};
    z[nt] = a;
#pragma unroll
    for (int s = 0; s < 4; ++s) {
      const bf16x8 hb = lds_ld<bf16x8>(lds, L_HT + (16 * nt + l15) * 256 + (((4 * s + g4) ^ l15) * 16));
      a = MFMA16(__builtin_bit_cast(bf16x8, w1f[s]), hb, a);
      z[nt] = MFMA16(__builtin_bit_cast(bf16x8, whf[s]), hb, z[nt]);
    }
    const u32x2 o = {pk_bf16(a[0] * CL2, a[1] * CL2), pk_bf16(a[2] * CL2, a[3] * CL2)};
    lds_st<u32x2>(lds, L_AIMG + (16 * nt + l15) * AP + (16 * w + 4 * g4) * 2, o);
  }

  auto geometry = [&](int i, u32 en, u32 nl, u32x2 p2, u32 eat) {
    const u32 s6 = (en & 0x3fffu) * 6u;
    const unsigned long long pv = (((unsigned long long)p2[1] << 32) | p2[0]) >> ((s6 & 2u) * 8u);
    const u32 plo = (u32)pv, phi = (u32)(pv >> 32);
    const f32x4 pd = lds_ld<f32x4>(lds, L_POS + nl * 16);
    const u32 dxy = pk_bf16(pd[0] - lo_f(plo), pd[1] - hi_f(plo));
    const u32 dz0 = pk_bf16(pd[2] - lo_f(phi), 0.f);
    const float dx = lo_f(dxy), dy = hi_f(dxy), dz = lo_f(dz0);
    const u32 sxy = pk_bf16(dx * dx, dy * dy);
    const u32 sz0 = pk_bf16(dz * dz, 0.f);
    const u32 d2 = pk_bf16(lo_f(sxy) + hi_f(sxy) + lo_f(sz0), 0.f);
    const u32x2 ra = {dxy, (dz0 & 0xffffu) | (((nl - (u32)g_n) * 2u) << 16)};
    lds_st<u32x2>(lds, L_RECA + i * 8, ra);
    lds_st<u32>(lds, L_RECB + i * 4, (d2 & 0xffffu) | (eat << 16));
  };
  auto build_s = [&](int base, int end) {
    const u32x4 zz = {0, 0, 0, 0};
    lds_st<u32x4>(lds, sbase + lane * 16, zz);
    lds_st<u32x4>(lds, sbase + 1024 + lane * 16, zz);
    const int idx = base + r;
    const bool valid = idx < end;
    if (hh == 0) {
      if (valid) {
        const u32x2 a = lds_ld<u32x2>(lds, L_RECA + idx * 8);
        const int ad = sbase + r * 64 + (int)(a[1] >> 16);
        lds_st<u16>(lds, ad, (u16)0x3f80);
        lds_st<u16>(lds, ad + 16, (u16)a[0]);
        lds_st<u16>(lds, ad + 32, (u16)(a[0] >> 16));
        lds_st<u16>(lds, ad + 48, (u16)a[1]);
      }
    } else {
      const u32 v = valid ? lds_ld<u32>(lds, L_RECB + idx * 4) : 0u;
      const u32x4 o = {v, v, 0, 0};
      lds_st<u32x4>(lds, sbase + 2048 + r * 16, o);
    }
  };

  asm volatile("s_waitcnt vmcnt(8)" : "+v"(ps[0]), "+v"(ps[1]), "+v"(eav[0]), "+v"(eav[1]), "+v"(wh2[0]), "+v"(wh2[1]),
                                      "+v"(wh2[2]), "+v"(wh2[3]) :: "memory");
  STAMP(4);
  {
#pragma unroll
    for (int rd = 0; rd < 2; ++rd) {
      const int i = eb + lane + 64 * rd;
      if (i < ee) geometry(i, ent[rd], nlv[rd], ps[rd], eav[rd]);
    }
    for (int i = eb + 128 + lane; i < ee; i += 64) {
      const u32 en = lds_ld<u32>(lds, L_LIST + i * 4);
      const u32 nl = lds_ld<unsigned char>(lds, L_NL + i);
      const u32 s6 = (en & 0x3fffu) * 6u;
      const u32x2 p2 = *(const u32x2*)((const char*)pos + (s6 & ~3u));
      geometry(i, en, nl, p2, ea[en >> 14]);
    }
    if (ee > eb) build_s(eb, ee);
  }
  wg_barrier();
  STAMP(5);

  f32x16 acc[4];
#pragma unroll
  for (int ct = 0; ct < 4; ++ct)
#pragma unroll
    for (int i = 0; i < 16; ++i) acc[ct][i] = 0.f;
  const int comp = r >> 3, nlr = r & 7;
  if (!slow) {
    bf16x8 id[2];
#pragma unroll
    for (int s = 0; s < 2; ++s) {
      const int dlt = r - 16 * s - 8 * hh;
      u32x4 v;
#pragma unroll
      for (int i = 0; i < 4; ++i) v[i] = dlt == 2 * i ? 0x3f80u : (dlt == 2 * i + 1 ? 0x3f800000u : 0u);
      id[s] = __builtin_bit_cast(bf16x8, v);
    }
    const f32x2 cc = {CL2, CL2};
    int boff[2];
#pragma unroll
    for (int c1 = 0; c1 < 2; ++c1)
      boff[c1] = rowbuf + hh * 1024 + tq * 128 + (((4 * c1 + 2 * (g4 & 1) + (tp >> 1)) ^ ((tq >> 1) << 2)) * 16) + (tp & 1) * 8;
    const int soff = sbase + (4 * hh + tq) * 64 + (16 * (g4 & 1) + 4 * tp) * 2;
    const int eoff = hh ? sbase + 2048 + r * 16 : sbase + r * 64;
    bf16x8 ebf[4];
    {
      const int colo = (16 * (g4 & 1) + 4 * tp) * 2;
      const int a1 = L_AIMG + (hh ? 48 + tq : g_n + tq) * AP + colo;
      const int a2 = L_AIMG + (hh ? 52 : g_n + 4 + tq) * AP + colo;
#pragma unroll
      for (int ct = 0; ct < 4; ++ct) ebf[ct] = cat4(lds_tr(lds, a1 + 64 * ct), lds_tr(lds, a2 + 64 * ct));
    }
    for (int base = eb; base < ee; base += 32) {
      Frags f;
      TICK0;
      asm volatile("s_waitcnt vmcnt(0)" ::: "memory");
      if (base == eb) TICK(0); else TICK(1);
      f.e = lds_ld<bf16x8>(lds, eoff);
#pragma unroll
      for (int ct = 0; ct < 4; ++ct)
#pragma unroll
        for (int s = 0; s < 2; ++s) {
          const int o = boff[ct & 1] + (ct >> 1) * 4096 + s * 2048;
          f.b[ct][s] = cat4(lds_tr(lds, o), lds_tr(lds, o + 512));
        }
      f.s[0] = cat4(lds_tr(lds, soff), lds_tr(lds, soff + 512));
      f.s[1] = cat4(lds_tr(lds, soff + 1024), lds_tr(lds, soff + 1536));
      asm volatile("" : "+v"(f.b[0][0]), "+v"(f.b[0][1]), "+v"(f.b[1][0]), "+v"(f.b[1][1]), "+v"(f.b[2][0]), "+v"(f.b[2][1]),
                        "+v"(f.b[3][0]), "+v"(f.b[3][1]), "+v"(f.s[0]), "+v"(f.s[1]), "+v"(f.e) :: "memory");
      const int rem = ee - base;
      if (rem > 32) {
        issue_rows(base + 32, ee - 1, 0x3fffu);
        build_s(base + 32, ee);
      }
      TICK(2);
      block_compute(f, id, ebf, acc, cc, rem);
      TICK(3);
    }
    asm volatile("s_nop 11" : "+v"(acc[0]), "+v"(acc[1]), "+v"(acc[2]), "+v"(acc[3]));
    if (comp == 0 && nlr < g_cnt) {
      const int ad = L_MIMG + (g_n + nlr) * MP + 8 * hh;
#pragma unroll
      for (int ct = 0; ct < 4; ++ct)
#pragma unroll
        for (int g = 0; g < 4; ++g) {
          const u32x2 v = {pk_bf16(acc[ct][4 * g], acc[ct][4 * g + 1]), pk_bf16(acc[ct][4 * g + 2], acc[ct][4 * g + 3])};
          lds_st<u32x2>(lds, ad + (32 * ct + 8 * g) * 2, v);
        }
    }
  } else {
    asm volatile("s_waitcnt vmcnt(0)" ::: "memory");
    __syncthreads();
    float* macc = (float*)(lds + L_ROWS);
    float* pacc = (float*)(lds + L_ROWS + 20480);
    for (int i = tid; i < 40 * 128 + 160; i += 512) macc[i] = 0.f;
    __syncthreads();
    const u32 wd2 = *(const u32*)(we + 256 * 128 + 2 * lane), wa2 = *(const u32*)(we + 257 * 128 + 2 * lane), wx2 = *(const u32*)(wx + 2 * lane);
    for (int e0 = w * 64; e0 < EE; e0 += 512) {
      const int dv = dstg[e0 + lane] - n0;
      unsigned long long m = __ballot((unsigned)dv < (unsigned)nn);
      while (m) {
        const int bit = __ffsll((long long)m) - 1;
        m &= m - 1;
        const int e = e0 + bit;
        const int nl = __builtin_amdgcn_readlane(dv, bit);
        const int s = srcg[e];
        float df[3], d2 = 0.f;
#pragma unroll
        for (int i = 0; i < 3; ++i) {
          df[i] = lo_f(pk_bf16(lds_ld<float>(lds, L_POS + nl * 16 + i * 4) - bf2f(pos[s * 3 + i]), 0.f));
          d2 += lo_f(pk_bf16(df[i] * df[i], 0.f));
        }
        d2 = lo_f(pk_bf16(d2, 0.f)) * CL2;
        const float eat = bf2f(ea[e]) * CL2;
        const u32 a2 = lds_ld<u32>(lds, L_AIMG + nl * AP + lane * 4);
        const u32 b2 = *(const u32*)(bp + (size_t)s * 256 + lane * 4);
        const float m0 = lo_f(act1(lo_f(a2) + lo_f(b2) + d2 * lo_f(wd2) + eat * lo_f(wa2)));
        const float m1 = lo_f(act1(hi_f(a2) + hi_f(b2) + d2 * hi_f(wd2) + eat * hi_f(wa2)));
        atomicAdd(&macc[nl * 128 + 2 * lane], m0);
        atomicAdd(&macc[nl * 128 + 2 * lane + 1], m1);
        float t = m0 * lo_f(wx2) + m1 * hi_f(wx2);
#pragma unroll
        for (int o = 32; o > 0; o >>= 1) t += __shfl_xor(t, o);
        if (lane < 3) atomicAdd(&pacc[nl * 4 + lane], (lane == 0 ? df[0] : lane == 1 ? df[1] : df[2]) * t);
      }
    }
    __syncthreads();
    for (int i = tid; i < 40 * 64; i += 512) {
      const int nl = i >> 6, c2 = i & 63;
      lds_st<u32>(lds, L_MIMG + nl * MP + c2 * 4, pk_bf16(macc[nl * 128 + 2 * c2], macc[nl * 128 + 2 * c2 + 1]));
    }
    if (tid < 3 * nn) {
      const int i = tid / 3, cc = tid - 3 * i;
      out[NN * 128 + n0 * 3 + tid] = (u16)pk_bf16(lds_ld<float>(lds, L_POS + i * 16 + cc * 4) + pacc[i * 4 + cc], 0.f);
    }
  }
  STAMP(6);
  wg_barrier();
  STAMP(7);

#pragma unroll
  for (int nt = 0; nt < 3; ++nt) {
#pragma unroll
    for (int s = 0; s < 4; ++s) {
      const bf16x8 mb = lds_ld<bf16x8>(lds, L_MIMG + (16 * nt + l15) * MP + (32 * s + 8 * g4) * 2);
      z[nt] = MFMA16(__builtin_bit_cast(bf16x8, wh2[s]), mb, z[nt]);
    }
    const u32x2 hres = lds_ld<u32x2>(lds, L_HT + (16 * nt + l15) * 256 + (((2 * w + (g4 >> 1)) ^ l15) * 16) + (g4 & 1) * 8);
    float o[4];
#pragma unroll
    for (int i = 0; i < 4; ++i) {
      const float zz = z[nt][i];
      const float sg = __builtin_amdgcn_rcpf(1.f + __builtin_amdgcn_exp2f(zz * CL2));
      const float hv = (i & 1) ? hi_f(hres[i >> 1]) : lo_f(hres[i >> 1]);
      o[i] = hv + zz * sg;
    }
    if (16 * nt + l15 < nn) {
      const u32x2 ov = {pk_bf16(o[0], o[1]), pk_bf16(o[2], o[3])};
      *(u32x2*)(out + (size_t)(n0 + 16 * nt + l15) * 128 + 16 * w + 4 * g4) = ov;
    }
  }
  if (!slow) {
    f32x2 t = {0.f, 0.f};
#pragma unroll
    for (int ct = 0; ct < 4; ++ct)
#pragma unroll
      for (int g = 0; g < 4; ++g) {
        const f32x4 wv = lds_ld<f32x4>(lds, L_WX + (32 * ct + 8 * g + 4 * hh) * 4);
        const f32x2 a0 = {acc[ct][4 * g], acc[ct][4 * g + 1]}, a1 = {acc[ct][4 * g + 2], acc[ct][4 * g + 3]};
        const f32x2 w0 = {wv[0], wv[1]}, w1 = {wv[2], wv[3]};
        t = __builtin_elementwise_fma(a0, w0, t);
        t = __builtin_elementwise_fma(a1, w1, t);
      }
    float u = t[0] + t[1];
    u += __shfl_xor(u, 32);
    if (comp > 0 && hh == 0 && nlr < g_cnt) {
      const float p0 = lds_ld<float>(lds, L_POS + (g_n + nlr) * 16 + (comp - 1) * 4);
      out[NN * 128 + (n0 + g_n + nlr) * 3 + comp - 1] = (u16)pk_bf16(p0 + u, 0.f);
    }
  }
  STAMP(8);
  STAMP_OUT;
}

extern "C" void kernel_launch(void* const* d_in, const int* in_sizes, int n_in, void* d_out, int out_size, void* d_ws,
                              size_t ws_size, hipStream_t stream) {
  const u16* h = (const u16*)d_in[0];
  const u16* pos = (const u16*)d_in[1];
  const u16* ea = (const u16*)d_in[2];
  const int* src = (const int*)d_in[3];
  const int* dst = (const int*)d_in[4];
  const u16* we = (const u16*)d_in[5];
  const u16* wx = (const u16*)d_in[6];
  const u16* wh = (const u16*)d_in[7];
  k_pre<<<NPROD + NPACKB + NBIN, 512, 0, stream>>>(src, dst, h, we, wh, (char*)d_ws);
  k_main<<<NBIN, 512, 0, stream>>>(h, pos, ea, src, dst, we, wx, (const char*)d_ws, (u16*)d_out
  );
}
```

```cpp
#include <hip/hip_runtime.h>
#include <hip/hip_bf16.h>
#include <stdint.h>

typedef unsigned short u16;
typedef unsigned int u32;
typedef __attribute__((ext_vector_type(8))) short bf16x8;
typedef __attribute__((ext_vector_type(4))) short s16x4;
typedef __attribute__((ext_vector_type(16))) float f32x16;
typedef __attribute__((ext_vector_type(4))) float f32x4;
typedef __attribute__((ext_vector_type(2))) float f32x2;
typedef __attribute__((ext_vector_type(2))) __bf16 bf16x2_t;
typedef __attribute__((ext_vector_type(4))) u32 u32x4;
typedef __attribute__((ext_vector_type(2))) u32 u32x2;
template <int V> struct IC { static constexpr int value = V; };
#define LDSP __attribute__((address_space(3)))
#define DI __device__ __forceinline__

constexpr int NN = 10000, EE = 160000;
constexpr int NBIN = 256, CHUNK = 625, CAP = 14;
constexpr u32 WS_SLOTS = 0;
constexpr u32 WS_BP = 4u << 20;
constexpr u32 WS_FLAG = 7u << 20;
constexpr u32 WS_GEN = WS_FLAG + 4096;
constexpr float CL2 = -1.4426950408889634f;
constexpr int LCAP = 1024;
constexpr int SPIN_MAX = 20000;

constexpr int MP = 272, AP = 264;
constexpr int L_MIMG = 0;
constexpr int L_HT = L_MIMG + 40 * MP;
constexpr int L_LIST = L_HT + 48 * 256;
constexpr int L_BK = L_LIST + (LCAP + 320) * 8;
constexpr int L_POS = L_BK + 48 * 256;
constexpr int L_WX = L_POS + 40 * 16;
constexpr int L_CNT = L_WX + 512;
constexpr int L_DUM = L_CNT + 256;
constexpr int L_AIMG = L_DUM + 512;
constexpr int L_IMG = L_LIST;
constexpr int L_S = (L_AIMG + 53 * AP + 1023) / 1024 * 1024;
constexpr int L_ROWS = L_S + 32768;
constexpr int L_JUNK = L_ROWS + 65536;
constexpr int L_TOTAL = L_JUNK + 1024;
static_assert(L_S <= 65536 && L_POS - L_IMG >= 16384, "lds");
static_assert(L_TOTAL <= 163840, "lds");

DI float bf2f(u32 u) { return __uint_as_float(u << 16); }
DI u32 pk_bf16(float a, float b) { f32x2 v = {a, b}; return __builtin_bit_cast(u32, __builtin_convertvector(v, bf16x2_t)); }
DI float lo_f(u32 p) { return __uint_as_float(p << 16); }
DI float hi_f(u32 p) { return __uint_as_float(p & 0xffff0000u); }
DI int node0_of(int b) { return (625 * b + 15) >> 4; }


#define MFMA16(a, b, c) __builtin_amdgcn_mfma_f32_16x16x32_bf16((a), (b), (c), 0, 0, 0)
template <typename T> DI T lds_ld(char* lds, int off) { return *(T*)(lds + off); }
template <typename T> DI void lds_st(char* lds, int off, T v) { *(T*)(lds + off) = v; }
DI s16x4 lds_tr(char* lds, int off) { return __builtin_amdgcn_ds_read_tr16_b64_v4i16((LDSP s16x4*)(lds + off)); }
DI bf16x8 cat4(s16x4 a, s16x4 b) { return __builtin_shufflevector(a, b, 0, 1, 2, 3, 4, 5, 6, 7); }
DI void wg_barrier() { asm volatile("s_waitcnt lgkmcnt(0)\n\ts_barrier" ::: "memory"); }
#define VMWAIT(n) asm volatile("s_waitcnt vmcnt(" #n ")" ::: "memory")

DI void gld4(u32& d, const void* sbase, u32 voff) { asm volatile("global_load_dword %0, %1, %2" : "=v"(d) : "v"(voff), "s"(sbase) : "memory"); }
DI void gld4c(u32& d, const void* sbase, u32 voff) { asm volatile("global_load_dword %0, %1, %2 sc1" : "=v"(d) : "v"(voff), "s"(sbase) : "memory"); }
DI void gld16c(u32x4& d, const void* sbase, u32 voff) { asm volatile("global_load_dwordx4 %0, %1, %2 sc1" : "=v"(d) : "v"(voff), "s"(sbase) : "memory"); }
DI void gld16c2w(u32x4& d0, u32x4& d1, const void* sbase, u32 v0, u32 v1) {
  asm volatile("global_load_dwordx4 %0, %2, %4 sc1\n\tglobal_load_dwordx4 %1, %3, %4 sc1\n\ts_waitcnt vmcnt(0)"
               : "=&v"(d0), "=&v"(d1) : "v"(v0), "v"(v1), "s"(sbase) : "memory");
}
DI void gld16c4w(u32x4 (&d)[4], const void* sbase, u32 v0) {
  asm volatile("global_load_dwordx4 %0, %4, %6 offset:-4096 sc1\n\tglobal_load_dwordx4 %1, %4, %6 sc1\n\t"
               "global_load_dwordx4 %2, %5, %6 offset:-4096 sc1\n\tglobal_load_dwordx4 %3, %5, %6 sc1\n\ts_waitcnt vmcnt(0)"
               : "=&v"(d[0]), "=&v"(d[1]), "=&v"(d[2]), "=&v"(d[3]) : "v"(v0 + 4096u), "v"(v0 + 12288u), "s"(sbase) : "memory");
}
DI u32 lds_peek(char* lds, int off) { return __builtin_amdgcn_readfirstlane((int)*(volatile LDSP u32*)(lds + off)); }
DI void gld8(u32x2& d, const void* sbase, u32 voff) { asm volatile("global_load_dwordx2 %0, %1, %2" : "=v"(d) : "v"(voff), "s"(sbase) : "memory"); }
DI void gld2(u32& d, const void* sbase, u32 voff) { asm volatile("global_load_ushort %0, %1, %2" : "=v"(d) : "v"(voff), "s"(sbase) : "memory"); }
DI void gld2p(u32& d, const void* p) { asm volatile("global_load_ushort %0, %1, off" : "=v"(d) : "v"(p) : "memory"); }
DI void gld2hi(u32& d, const void* sbase, u32 voff) { asm volatile("global_load_short_d16_hi %0, %1, %2" : "=v"(d) : "v"(voff), "s"(sbase) : "memory"); }
DI void dma_rows(const void* gbase, u32 v0, u32 v1, u32 v2, u32 v3, u32 ldsb) {
  u32 keep;
  asm volatile(
      "s_mov_b32 %0, m0\n\t"
      "s_mov_b32 m0, %6\n\ts_nop 0\n\tglobal_load_lds_dwordx4 %1, %5 sc1\n\t"
      "s_add_u32 m0, %6, 1024\n\ts_nop 0\n\tglobal_load_lds_dwordx4 %2, %5 sc1\n\t"
      "s_add_u32 m0, %6, 2048\n\ts_nop 0\n\tglobal_load_lds_dwordx4 %3, %5 sc1\n\t"
      "s_add_u32 m0, %6, 3072\n\ts_nop 0\n\tglobal_load_lds_dwordx4 %4, %5 sc1\n\t"
      "s_add_u32 m0, %6, 3968\n\ts_nop 0\n\tglobal_load_lds_dwordx4 %1, %5 offset:128 sc1\n\t"
      "s_add_u32 m0, %6, 4992\n\ts_nop 0\n\tglobal_load_lds_dwordx4 %2, %5 offset:128 sc1\n\t"
      "s_add_u32 m0, %6, 6016\n\ts_nop 0\n\tglobal_load_lds_dwordx4 %3, %5 offset:128 sc1\n\t"
      "s_add_u32 m0, %6, 7040\n\ts_nop 0\n\tglobal_load_lds_dwordx4 %4, %5 offset:128 sc1\n\t"
      "s_mov_b32 m0, %0"
      : "=&s"(keep)
      : "v"(v0), "v"(v1), "v"(v2), "v"(v3), "s"(gbase), "s"(ldsb)
      : "memory", "scc");
}
DI void gst16c(const void* sbase, u32 voff, u32x4 v) { asm volatile("global_store_dwordx4 %0, %1, %2 sc1\n\ts_nop 1" :: "v"(voff), "v"(v), "s"(sbase) : "memory"); }
DI void gst16(const void* sbase, u32 voff, u32x4 v) { asm volatile("global_store_dwordx4 %0, %1, %2\n\ts_nop 1" :: "v"(voff), "v"(v), "s"(sbase) : "memory"); }
DI void gst8c(const void* sbase, u32 voff, u32x2 v) { asm volatile("global_store_dwordx2 %0, %1, %2 sc1" :: "v"(voff), "v"(v), "s"(sbase) : "memory"); }

DI void dma1(const void* gbase, u32 voff, u32 ldsb) {
  u32 keep;
  asm volatile("s_mov_b32 %0, m0\n\ts_mov_b32 m0, %3\n\ts_nop 0\n\tglobal_load_lds_dwordx4 %1, %2\n\ts_mov_b32 m0, %0"
               : "=&s"(keep) : "v"(voff), "s"(gbase), "s"(ldsb) : "memory");
}
DI void dma4(const void* gbase, u32 v0, u32 v1, u32 ldsb) {
  u32 keep;
  asm volatile("s_mov_b32 %0, m0\n\ts_mov_b32 m0, %4\n\ts_nop 0\n\tglobal_load_lds_dwordx4 %1, %3\n\t"
               "global_load_lds_dwordx4 %1, %3 offset:1024\n\tglobal_load_lds_dwordx4 %2, %3 offset:2048\n\t"
               "global_load_lds_dwordx4 %2, %3 offset:3072\n\ts_mov_b32 m0, %0"
               : "=&s"(keep) : "v"(v0), "v"(v1), "s"(gbase), "s"(ldsb) : "memory");
}
#define LOOP_ASM \
  "s_waitcnt lgkmcnt(0)\n" \
  "s_setprio 3\n" \
  "s_mov_b32 s60, %12\n" \
  "v_mov_b32 v206, %25\n" \
  "v_mov_b32 v207, %26\n" \
  "v_mov_b32 v198, 0\n" \
  "v_mov_b32 v199, 0\n" \
  "v_mov_b32 v200, 0\n" \
  "v_mov_b32 v201, 0\n" \
  "v_mov_b32 v254, 0\n" \
  "v_mov_b32 v255, 0\n" \
  "v_lshrrev_b32 v211, 19, v207\n" \
  "v_lshlrev_b32 v212, 4, v211\n" \
  "ds_read_b96 v[214:216], v212 offset:%29\n" \
  "ds_write2st64_b64 %18, v[254:255], v[254:255] offset0:0 offset1:1\n" \
  "ds_write2st64_b64 %18, v[254:255], v[254:255] offset0:2 offset1:3\n" \
  "ds_write_b64 %18, v[254:255] offset:2048\n" \
  "v_lshrrev_b32 v218, 4, v206\n" \
  "v_and_b32 v218, 16, v218\n" \
  "v_lshl_add_u32 v221, v211, 1, %19\n" \
  "s_mov_b32 s62, m0\n" \
  "s_min_u32 s61, s60, 32\n" \
  "s_bfm_b64 s[64:65], s61, 0\n" \
  "s_mov_b32 s66, 0xbfb8aa3b\n" \
  "s_mov_b32 s67, 0xbfb8aa3b\n" \
  "s_cmp_lt_i32 s60, 1\n" \
  "v_mfma_f32_32x32x16_bf16 %0, v[198:201], v[198:201], 0\n" \
  "v_mfma_f32_32x32x16_bf16 %1, v[198:201], v[198:201], 0\n" \
  "v_mfma_f32_32x32x16_bf16 %2, v[198:201], v[198:201], 0\n" \
  "v_mfma_f32_32x32x16_bf16 %3, v[198:201], v[198:201], 0\n" \
  "s_cbranch_scc1 .Lend_%=\n" \
  "s_waitcnt vmcnt(8)\n" \
  "v_mov_b64 v[208:209], %27\n" \
  "v_mov_b32 v210, %28\n" \
  "s_branch .Lh1_%=\n" \
  ".Lhead_%=:\n" \
  "v_lshrrev_b32 v211, 19, v207\n" \
  "v_lshlrev_b32 v212, 4, v211\n" \
  "ds_read_b96 v[214:216], v212 offset:%29\n" \
  "ds_write2st64_b64 %18, v[254:255], v[254:255] offset0:0 offset1:1\n" \
  "ds_write2st64_b64 %18, v[254:255], v[254:255] offset0:2 offset1:3\n" \
  "ds_write_b64 %18, v[254:255] offset:2048\n" \
  "v_lshrrev_b32 v218, 4, v206\n" \
  "v_and_b32 v218, 16, v218\n" \
  "v_lshl_add_u32 v221, v211, 1, %19\n" \
  "s_waitcnt vmcnt(8)\n" \
  ".Lh1_%=:\n" \
  "s_waitcnt lgkmcnt(3)\n" \
  "v_alignbit_b32 v219, v209, v208, v218\n" \
  "v_lshrrev_b32 v220, v218, v209\n" \
  "v_lshlrev_b32 v218, 16, v219\n" \
  "v_and_b32 v219, 0xffff0000, v219\n" \
  "v_lshlrev_b32 v220, 16, v220\n" \
  "v_sub_f32 v218, v214, v218\n" \
  "v_sub_f32 v219, v215, v219\n" \
  "v_sub_f32 v220, v216, v220\n" \
  "v_cvt_pk_bf16_f32 v208, v218, v219\n" \
  "v_cvt_pk_bf16_f32 v209, v220, 1.0\n" \
  "v_lshlrev_b32 v218, 16, v208\n" \
  "v_and_b32 v219, 0xffff0000, v208\n" \
  "v_lshlrev_b32 v220, 16, v209\n" \
  "v_mul_f32 v218, v218, v218\n" \
  "v_mul_f32 v219, v219, v219\n" \
  "v_mul_f32 v220, v220, v220\n" \
  "v_cvt_pk_bf16_f32 v218, v218, v219\n" \
  "v_cvt_pk_bf16_f32 v220, v220, 0\n" \
  "v_lshlrev_b32 v219, 16, v218\n" \
  "v_and_b32 v218, 0xffff0000, v218\n" \
  "v_lshlrev_b32 v220, 16, v220\n" \
  "v_add_f32 v218, v219, v218\n" \
  "v_and_b32 v210, 0xffff0000, v210\n" \
  "v_add_f32 v218, v218, v220\n" \
  "v_cvt_pk_bf16_f32 v218, v218, 0\n" \
  "v_or_b32 v214, v218, v210\n" \
  "v_or_b32 v215, v218, v210\n" \
  "s_mov_b64 exec, s[64:65]\n" \
  "ds_write_b64 %20, v[214:215]\n" \
  "ds_write_b16_d16_hi v221, v209\n" \
  "ds_write_b16 v221, v208 offset:16\n" \
  "ds_write_b16_d16_hi v221, v208 offset:32\n" \
  "ds_write_b16 v221, v209 offset:48\n" \
  "s_mov_b64 exec, -1\n" \
  "ds_read_b128 v[246:249], %16\n" \
  "ds_read_b64_tr_b16 v[238:239], %15 offset:0\n" \
  "ds_read_b64_tr_b16 v[240:241], %15 offset:512\n" \
  "ds_read_b64_tr_b16 v[242:243], %15 offset:1024\n" \
  "ds_read_b64_tr_b16 v[244:245], %15 offset:1536\n" \
  "ds_read_b32 v250, %4 offset:0\n" \
  "ds_read_b32 v251, %4 offset:64\n" \
  "ds_read_b32 v252, %4 offset:128\n" \
  "ds_read_b32 v253, %4 offset:192\n" \
  "s_waitcnt vmcnt(4)\n" \
  "ds_read_b64_tr_b16 v[206:207], %13 offset:0\n" \
  "ds_read_b64_tr_b16 v[208:209], %13 offset:512\n" \
  "ds_read_b64_tr_b16 v[210:211], %13 offset:2048\n" \
  "ds_read_b64_tr_b16 v[212:213], %13 offset:2560\n" \
  "ds_read_b64_tr_b16 v[214:215], %14 offset:0\n" \
  "ds_read_b64_tr_b16 v[216:217], %14 offset:512\n" \
  "ds_read_b64_tr_b16 v[218:219], %14 offset:2048\n" \
  "ds_read_b64_tr_b16 v[220:221], %14 offset:2560\n" \
  "s_waitcnt lgkmcnt(15)\n" \
  "v_mfma_f32_32x32x16_bf16 v[150:165], v[246:249], %8, 0\n" \
  "s_waitcnt lgkmcnt(6)\n" \
  "v_mfma_f32_32x32x16_bf16 v[150:165], %6, v[206:209], v[150:165]\n" \
  "s_waitcnt lgkmcnt(4)\n" \
  "v_mfma_f32_32x32x16_bf16 v[150:165], %7, v[210:213], v[150:165]\n" \
  "s_waitcnt vmcnt(0)\n" \
  "ds_read_b64_tr_b16 v[222:223], %13 offset:4096\n" \
  "ds_read_b64_tr_b16 v[224:225], %13 offset:4608\n" \
  "ds_read_b64_tr_b16 v[226:227], %13 offset:6144\n" \
  "ds_read_b64_tr_b16 v[228:229], %13 offset:6656\n" \
  "ds_read_b64_tr_b16 v[230:231], %14 offset:4096\n" \
  "ds_read_b64_tr_b16 v[232:233], %14 offset:4608\n" \
  "ds_read_b64_tr_b16 v[234:235], %14 offset:6144\n" \
  "ds_read_b64_tr_b16 v[236:237], %14 offset:6656\n" \
  "ds_read_b64 v[206:207], %5\n" \
  "s_cmp_gt_i32 s60, 32\n" \
  "s_cbranch_scc0 .Llast_%=\n" \
  "s_cmp_gt_i32 s60, 64\n" \
  "s_cbranch_scc1 .Lp3_%=\n" \
  "s_setprio 2\n" \
  ".Lp3_%=:\n" \
  "s_sub_u32 s60, s60, 32\n" \
  "s_min_u32 s61, s60, 32\n" \
  "s_bfm_b64 s[64:65], s61, 0\n" \
  "v_exp_f32 v182, v150\n" \
  "s_waitcnt lgkmcnt(0)\n" \
  "v_lshrrev_b32 v211, 8, v206\n" \
  "v_mul_u32_u24 v211, 6, v211\n" \
  "v_and_b32 v211, -4, v211\n" \
  "v_and_b32 v212, 0x7ffff, v207\n" \
  "v_exp_f32 v183, v151\n" \
  "v_or_b32 v250, v250, %17\n" \
  "v_or_b32 v251, v251, %17\n" \
  "v_or_b32 v252, v252, %17\n" \
  "v_or_b32 v253, v253, %17\n" \
  "v_exp_f32 v184, v152\n" \
  "v_mfma_f32_32x32x16_bf16 v[166:181], v[246:249], %9, 0\n" \
  "v_exp_f32 v185, v153\n" \
  "s_mov_b64 exec, s[64:65]\n" \
  "global_load_dwordx2 v[208:209], v211, %23\n" \
  "global_load_short_d16_hi v210, v212, %24\n" \
  "s_mov_b64 exec, -1\n" \
  "v_exp_f32 v186, v154\n" \
  "v_exp_f32 v187, v155\n" \
  "v_mfma_f32_32x32x16_bf16 v[166:181], %6, v[214:217], v[166:181]\n" \
  "v_exp_f32 v188, v156\n" \
  "s_add_u32 m0, %22, 0\n" \
  "v_exp_f32 v189, v157\n" \
  "global_load_lds_dwordx4 v250, %21 sc1\n" \
  "v_exp_f32 v190, v158\n" \
  "v_mfma_f32_32x32x16_bf16 v[166:181], %7, v[218:221], v[166:181]\n" \
  "v_exp_f32 v191, v159\n" \
  "s_add_u32 m0, %22, 1024\n" \
  "v_exp_f32 v192, v160\n" \
  "global_load_lds_dwordx4 v251, %21 sc1\n" \
  "v_exp_f32 v193, v161\n" \
  "v_exp_f32 v194, v162\n" \
  "s_add_u32 m0, %22, 2048\n" \
  "v_exp_f32 v195, v163\n" \
  "global_load_lds_dwordx4 v252, %21 sc1\n" \
  "v_exp_f32 v196, v164\n" \
  "v_exp_f32 v197, v165\n" \
  "v_pk_fma_f32 v[182:183], v[182:183], s[66:67], s[66:67]\n" \
  "v_pk_fma_f32 v[184:185], v[184:185], s[66:67], s[66:67]\n" \
  "v_pk_fma_f32 v[186:187], v[186:187], s[66:67], s[66:67]\n" \
  "v_pk_fma_f32 v[188:189], v[188:189], s[66:67], s[66:67]\n" \
  "v_pk_fma_f32 v[190:191], v[190:191], s[66:67], s[66:67]\n" \
  "v_pk_fma_f32 v[192:193], v[192:193], s[66:67], s[66:67]\n" \
  "v_pk_fma_f32 v[194:195], v[194:195], s[66:67], s[66:67]\n" \
  "v_pk_fma_f32 v[196:197], v[196:197], s[66:67], s[66:67]\n" \
  "v_rcp_f32 v182, v182\n" \
  "v_rcp_f32 v183, v183\n" \
  "s_add_u32 m0, %22, 3072\n" \
  "v_rcp_f32 v184, v184\n" \
  "global_load_lds_dwordx4 v253, %21 sc1\n" \
  "v_rcp_f32 v185, v185\n" \
  "v_rcp_f32 v186, v186\n" \
  "s_add_u32 m0, %22, 3968\n" \
  "v_rcp_f32 v187, v187\n" \
  "global_load_lds_dwordx4 v250, %21 offset:128 sc1\n" \
  "v_rcp_f32 v188, v188\n" \
  "v_rcp_f32 v189, v189\n" \
  "s_add_u32 m0, %22, 4992\n" \
  "v_rcp_f32 v190, v190\n" \
  "global_load_lds_dwordx4 v251, %21 offset:128 sc1\n" \
  "v_rcp_f32 v191, v191\n" \
  "v_rcp_f32 v192, v192\n" \
  "s_add_u32 m0, %22, 6016\n" \
  "v_rcp_f32 v193, v193\n" \
  "global_load_lds_dwordx4 v252, %21 offset:128 sc1\n" \
  "v_rcp_f32 v194, v194\n" \
  "v_rcp_f32 v195, v195\n" \
  "v_add_u32 %4, 0x100, %4\n" \
  "v_add_u32 %5, 0x100, %5\n" \
  "v_rcp_f32 v196, v196\n" \
  "v_rcp_f32 v197, v197\n" \
  "v_pk_mul_f32 v[182:183], v[182:183], v[150:151]\n" \
  "v_pk_mul_f32 v[184:185], v[184:185], v[152:153]\n" \
  "v_pk_mul_f32 v[186:187], v[186:187], v[154:155]\n" \
  "v_pk_mul_f32 v[188:189], v[188:189], v[156:157]\n" \
  "v_pk_mul_f32 v[190:191], v[190:191], v[158:159]\n" \
  "v_pk_mul_f32 v[192:193], v[192:193], v[160:161]\n" \
  "v_pk_mul_f32 v[194:195], v[194:195], v[162:163]\n" \
  "v_pk_mul_f32 v[196:197], v[196:197], v[164:165]\n" \
  "v_cvt_pk_bf16_f32 v198, v182, v183\n" \
  "v_cvt_pk_bf16_f32 v199, v184, v185\n" \
  "v_cvt_pk_bf16_f32 v200, v186, v187\n" \
  "v_cvt_pk_bf16_f32 v201, v188, v189\n" \
  "v_cvt_pk_bf16_f32 v202, v190, v191\n" \
  "v_cvt_pk_bf16_f32 v203, v192, v193\n" \
  "v_cvt_pk_bf16_f32 v204, v194, v195\n" \
  "v_cvt_pk_bf16_f32 v205, v196, v197\n" \
  "v_exp_f32 v182, v166\n" \
  "s_add_u32 m0, %22, 7040\n" \
  "v_exp_f32 v183, v167\n" \
  "global_load_lds_dwordx4 v253, %21 offset:128 sc1\n" \
  "v_exp_f32 v184, v168\n" \
  "v_mfma_f32_32x32x16_bf16 %0, v[198:201], v[238:241], %0\n" \
  "v_exp_f32 v185, v169\n" \
  "v_exp_f32 v186, v170\n" \
  "v_exp_f32 v187, v171\n" \
  "v_mfma_f32_32x32x16_bf16 %0, v[202:205], v[242:245], %0\n" \
  "v_exp_f32 v188, v172\n" \
  "v_exp_f32 v189, v173\n" \
  "v_exp_f32 v190, v174\n" \
  "v_mfma_f32_32x32x16_bf16 v[150:165], v[246:249], %10, 0\n" \
  "v_exp_f32 v191, v175\n" \
  "v_exp_f32 v192, v176\n" \
  "v_exp_f32 v193, v177\n" \
  "v_mfma_f32_32x32x16_bf16 v[150:165], %6, v[222:225], v[150:165]\n" \
  "v_exp_f32 v194, v178\n" \
  "v_exp_f32 v195, v179\n" \
  "v_exp_f32 v196, v180\n" \
  "v_exp_f32 v197, v181\n" \
  "v_pk_fma_f32 v[182:183], v[182:183], s[66:67], s[66:67]\n" \
  "v_pk_fma_f32 v[184:185], v[184:185], s[66:67], s[66:67]\n" \
  "v_pk_fma_f32 v[186:187], v[186:187], s[66:67], s[66:67]\n" \
  "v_pk_fma_f32 v[188:189], v[188:189], s[66:67], s[66:67]\n" \
  "v_pk_fma_f32 v[190:191], v[190:191], s[66:67], s[66:67]\n" \
  "v_pk_fma_f32 v[192:193], v[192:193], s[66:67], s[66:67]\n" \
  "v_pk_fma_f32 v[194:195], v[194:195], s[66:67], s[66:67]\n" \
  "v_pk_fma_f32 v[196:197], v[196:197], s[66:67], s[66:67]\n" \
  "v_mfma_f32_32x32x16_bf16 v[150:165], %7, v[226:229], v[150:165]\n" \
  "v_rcp_f32 v182, v182\n" \
  "v_rcp_f32 v183, v183\n" \
  "v_rcp_f32 v184, v184\n" \
  "v_rcp_f32 v185, v185\n" \
  "v_rcp_f32 v186, v186\n" \
  "v_rcp_f32 v187, v187\n" \
  "v_rcp_f32 v188, v188\n" \
  "v_rcp_f32 v189, v189\n" \
  "v_rcp_f32 v190, v190\n" \
  "v_rcp_f32 v191, v191\n" \
  "v_rcp_f32 v192, v192\n" \
  "v_rcp_f32 v193, v193\n" \
  "v_rcp_f32 v194, v194\n" \
  "v_rcp_f32 v195, v195\n" \
  "v_rcp_f32 v196, v196\n" \
  "v_rcp_f32 v197, v197\n" \
  "v_pk_mul_f32 v[182:183], v[182:183], v[166:167]\n" \
  "v_pk_mul_f32 v[184:185], v[184:185], v[168:169]\n" \
  "v_pk_mul_f32 v[186:187], v[186:187], v[170:171]\n" \
  "v_pk_mul_f32 v[188:189], v[188:189], v[172:173]\n" \
  "v_pk_mul_f32 v[190:191], v[190:191], v[174:175]\n" \
  "v_pk_mul_f32 v[192:193], v[192:193], v[176:177]\n" \
  "v_pk_mul_f32 v[194:195], v[194:195], v[178:179]\n" \
  "v_pk_mul_f32 v[196:197], v[196:197], v[180:181]\n" \
  "v_cvt_pk_bf16_f32 v198, v182, v183\n" \
  "v_cvt_pk_bf16_f32 v199, v184, v185\n" \
  "v_cvt_pk_bf16_f32 v200, v186, v187\n" \
  "v_cvt_pk_bf16_f32 v201, v188, v189\n" \
  "v_cvt_pk_bf16_f32 v202, v190, v191\n" \
  "v_cvt_pk_bf16_f32 v203, v192, v193\n" \
  "v_cvt_pk_bf16_f32 v204, v194, v195\n" \
  "v_cvt_pk_bf16_f32 v205, v196, v197\n" \
  "v_exp_f32 v182, v150\n" \
  "v_exp_f32 v183, v151\n" \
  "v_exp_f32 v184, v152\n" \
  "v_mfma_f32_32x32x16_bf16 %1, v[198:201], v[238:241], %1\n" \
  "v_exp_f32 v185, v153\n" \
  "v_exp_f32 v186, v154\n" \
  "v_exp_f32 v187, v155\n" \
  "v_mfma_f32_32x32x16_bf16 %1, v[202:205], v[242:245], %1\n" \
  "v_exp_f32 v188, v156\n" \
  "v_exp_f32 v189, v157\n" \
  "v_exp_f32 v190, v158\n" \
  "v_mfma_f32_32x32x16_bf16 v[166:181], v[246:249], %11, 0\n" \
  "v_exp_f32 v191, v159\n" \
  "v_exp_f32 v192, v160\n" \
  "v_exp_f32 v193, v161\n" \
  "v_mfma_f32_32x32x16_bf16 v[166:181], %6, v[230:233], v[166:181]\n" \
  "v_exp_f32 v194, v162\n" \
  "v_exp_f32 v195, v163\n" \
  "v_exp_f32 v196, v164\n" \
  "v_exp_f32 v197, v165\n" \
  "v_pk_fma_f32 v[182:183], v[182:183], s[66:67], s[66:67]\n" \
  "v_pk_fma_f32 v[184:185], v[184:185], s[66:67], s[66:67]\n" \
  "v_pk_fma_f32 v[186:187], v[186:187], s[66:67], s[66:67]\n" \
  "v_pk_fma_f32 v[188:189], v[188:189], s[66:67], s[66:67]\n" \
  "v_pk_fma_f32 v[190:191], v[190:191], s[66:67], s[66:67]\n" \
  "v_pk_fma_f32 v[192:193], v[192:193], s[66:67], s[66:67]\n" \
  "v_pk_fma_f32 v[194:195], v[194:195], s[66:67], s[66:67]\n" \
  "v_pk_fma_f32 v[196:197], v[196:197], s[66:67], s[66:67]\n" \
  "v_mfma_f32_32x32x16_bf16 v[166:181], %7, v[234:237], v[166:181]\n" \
  "v_rcp_f32 v182, v182\n" \
  "v_rcp_f32 v183, v183\n" \
  "v_rcp_f32 v184, v184\n" \
  "v_rcp_f32 v185, v185\n" \
  "v_rcp_f32 v186, v186\n" \
  "v_rcp_f32 v187, v187\n" \
  "v_rcp_f32 v188, v188\n" \
  "v_rcp_f32 v189, v189\n" \
  "v_rcp_f32 v190, v190\n" \
  "v_rcp_f32 v191, v191\n" \
  "v_rcp_f32 v192, v192\n" \
  "v_rcp_f32 v193, v193\n" \
  "v_rcp_f32 v194, v194\n" \
  "v_rcp_f32 v195, v195\n" \
  "v_rcp_f32 v196, v196\n" \
  "v_rcp_f32 v197, v197\n" \
  "v_pk_mul_f32 v[182:183], v[182:183], v[150:151]\n" \
  "v_pk_mul_f32 v[184:185], v[184:185], v[152:153]\n" \
  "v_pk_mul_f32 v[186:187], v[186:187], v[154:155]\n" \
  "v_pk_mul_f32 v[188:189], v[188:189], v[156:157]\n" \
  "v_pk_mul_f32 v[190:191], v[190:191], v[158:159]\n" \
  "v_pk_mul_f32 v[192:193], v[192:193], v[160:161]\n" \
  "v_pk_mul_f32 v[194:195], v[194:195], v[162:163]\n" \
  "v_pk_mul_f32 v[196:197], v[196:197], v[164:165]\n" \
  "v_cvt_pk_bf16_f32 v198, v182, v183\n" \
  "v_cvt_pk_bf16_f32 v199, v184, v185\n" \
  "v_cvt_pk_bf16_f32 v200, v186, v187\n" \
  "v_cvt_pk_bf16_f32 v201, v188, v189\n" \
  "v_cvt_pk_bf16_f32 v202, v190, v191\n" \
  "v_cvt_pk_bf16_f32 v203, v192, v193\n" \
  "v_cvt_pk_bf16_f32 v204, v194, v195\n" \
  "v_cvt_pk_bf16_f32 v205, v196, v197\n" \
  "v_exp_f32 v182, v166\n" \
  "v_exp_f32 v183, v167\n" \
  "v_exp_f32 v184, v168\n" \
  "v_mfma_f32_32x32x16_bf16 %2, v[198:201], v[238:241], %2\n" \
  "v_exp_f32 v185, v169\n" \
  "v_exp_f32 v186, v170\n" \
  "v_exp_f32 v187, v171\n" \
  "v_mfma_f32_32x32x16_bf16 %2, v[202:205], v[242:245], %2\n" \
  "v_exp_f32 v188, v172\n" \
  "v_exp_f32 v189, v173\n" \
  "v_exp_f32 v190, v174\n" \
  "v_exp_f32 v191, v175\n" \
  "v_exp_f32 v192, v176\n" \
  "v_exp_f32 v193, v177\n" \
  "v_exp_f32 v194, v178\n" \
  "v_exp_f32 v195, v179\n" \
  "v_exp_f32 v196, v180\n" \
  "v_exp_f32 v197, v181\n" \
  "v_pk_fma_f32 v[182:183], v[182:183], s[66:67], s[66:67]\n" \
  "v_pk_fma_f32 v[184:185], v[184:185], s[66:67], s[66:67]\n" \
  "v_pk_fma_f32 v[186:187], v[186:187], s[66:67], s[66:67]\n" \
  "v_pk_fma_f32 v[188:189], v[188:189], s[66:67], s[66:67]\n" \
  "v_pk_fma_f32 v[190:191], v[190:191], s[66:67], s[66:67]\n" \
  "v_pk_fma_f32 v[192:193], v[192:193], s[66:67], s[66:67]\n" \
  "v_pk_fma_f32 v[194:195], v[194:195], s[66:67], s[66:67]\n" \
  "v_pk_fma_f32 v[196:197], v[196:197], s[66:67], s[66:67]\n" \
  "v_rcp_f32 v182, v182\n" \
  "v_rcp_f32 v183, v183\n" \
  "v_rcp_f32 v184, v184\n" \
  "v_rcp_f32 v185, v185\n" \
  "v_rcp_f32 v186, v186\n" \
  "v_rcp_f32 v187, v187\n" \
  "v_rcp_f32 v188, v188\n" \
  "v_rcp_f32 v189, v189\n" \
  "v_rcp_f32 v190, v190\n" \
  "v_rcp_f32 v191, v191\n" \
  "v_rcp_f32 v192, v192\n" \
  "v_rcp_f32 v193, v193\n" \
  "v_rcp_f32 v194, v194\n" \
  "v_rcp_f32 v195, v195\n" \
  "v_rcp_f32 v196, v196\n" \
  "v_rcp_f32 v197, v197\n" \
  "v_pk_mul_f32 v[182:183], v[182:183], v[166:167]\n" \
  "v_pk_mul_f32 v[184:185], v[184:185], v[168:169]\n" \
  "v_pk_mul_f32 v[186:187], v[186:187], v[170:171]\n" \
  "v_pk_mul_f32 v[188:189], v[188:189], v[172:173]\n" \
  "v_pk_mul_f32 v[190:191], v[190:191], v[174:175]\n" \
  "v_pk_mul_f32 v[192:193], v[192:193], v[176:177]\n" \
  "v_pk_mul_f32 v[194:195], v[194:195], v[178:179]\n" \
  "v_pk_mul_f32 v[196:197], v[196:197], v[180:181]\n" \
  "v_cvt_pk_bf16_f32 v198, v182, v183\n" \
  "v_cvt_pk_bf16_f32 v199, v184, v185\n" \
  "v_cvt_pk_bf16_f32 v200, v186, v187\n" \
  "v_cvt_pk_bf16_f32 v201, v188, v189\n" \
  "v_cvt_pk_bf16_f32 v202, v190, v191\n" \
  "v_cvt_pk_bf16_f32 v203, v192, v193\n" \
  "v_cvt_pk_bf16_f32 v204, v194, v195\n" \
  "v_cvt_pk_bf16_f32 v205, v196, v197\n" \
  "s_setprio 3\n" \
  "s_nop 0\n" \
  "v_mfma_f32_32x32x16_bf16 %3, v[198:201], v[238:241], %3\n" \
  "v_mfma_f32_32x32x16_bf16 %3, v[202:205], v[242:245], %3\n" \
  "s_branch .Lhead_%=\n" \
  ".Llast_%=:\n" \
  "s_setprio 1\n" \
  "s_cmp_gt_i32 s60, 24\n" \
  "s_cbranch_scc0 .Lv3_%=\n" \
  "v_exp_f32 v182, v150\n" \
  "v_exp_f32 v183, v151\n" \
  "v_exp_f32 v184, v152\n" \
  "v_mfma_f32_32x32x16_bf16 v[166:181], v[246:249], %9, 0\n" \
  "v_exp_f32 v185, v153\n" \
  "v_exp_f32 v186, v154\n" \
  "v_exp_f32 v187, v155\n" \
  "s_waitcnt lgkmcnt(11)\n" \
  "v_mfma_f32_32x32x16_bf16 v[166:181], %6, v[214:217], v[166:181]\n" \
  "v_exp_f32 v188, v156\n" \
  "v_exp_f32 v189, v157\n" \
  "v_exp_f32 v190, v158\n" \
  "s_waitcnt lgkmcnt(9)\n" \
  "v_mfma_f32_32x32x16_bf16 v[166:181], %7, v[218:221], v[166:181]\n" \
  "v_exp_f32 v191, v159\n" \
  "v_exp_f32 v192, v160\n" \
  "v_exp_f32 v193, v161\n" \
  "v_exp_f32 v194, v162\n" \
  "v_exp_f32 v195, v163\n" \
  "v_exp_f32 v196, v164\n" \
  "v_exp_f32 v197, v165\n" \
  "v_pk_fma_f32 v[182:183], v[182:183], s[66:67], s[66:67]\n" \
  "v_pk_fma_f32 v[184:185], v[184:185], s[66:67], s[66:67]\n" \
  "v_pk_fma_f32 v[186:187], v[186:187], s[66:67], s[66:67]\n" \
  "v_pk_fma_f32 v[188:189], v[188:189], s[66:67], s[66:67]\n" \
  "v_pk_fma_f32 v[190:191], v[190:191], s[66:67], s[66:67]\n" \
  "v_pk_fma_f32 v[192:193], v[192:193], s[66:67], s[66:67]\n" \
  "v_pk_fma_f32 v[194:195], v[194:195], s[66:67], s[66:67]\n" \
  "v_pk_fma_f32 v[196:197], v[196:197], s[66:67], s[66:67]\n" \
  "v_rcp_f32 v182, v182\n" \
  "v_rcp_f32 v183, v183\n" \
  "v_rcp_f32 v184, v184\n" \
  "v_rcp_f32 v185, v185\n" \
  "v_rcp_f32 v186, v186\n" \
  "v_rcp_f32 v187, v187\n" \
  "v_rcp_f32 v188, v188\n" \
  "v_rcp_f32 v189, v189\n" \
  "v_rcp_f32 v190, v190\n" \
  "v_rcp_f32 v191, v191\n" \
  "v_rcp_f32 v192, v192\n" \
  "v_rcp_f32 v193, v193\n" \
  "v_rcp_f32 v194, v194\n" \
  "v_rcp_f32 v195, v195\n" \
  "v_rcp_f32 v196, v196\n" \
  "v_rcp_f32 v197, v197\n" \
  "v_pk_mul_f32 v[182:183], v[182:183], v[150:151]\n" \
  "v_pk_mul_f32 v[184:185], v[184:185], v[152:153]\n" \
  "v_pk_mul_f32 v[186:187], v[186:187], v[154:155]\n" \
  "v_pk_mul_f32 v[188:189], v[188:189], v[156:157]\n" \
  "v_pk_mul_f32 v[190:191], v[190:191], v[158:159]\n" \
  "v_pk_mul_f32 v[192:193], v[192:193], v[160:161]\n" \
  "v_pk_mul_f32 v[194:195], v[194:195], v[162:163]\n" \
  "v_pk_mul_f32 v[196:197], v[196:197], v[164:165]\n" \
  "v_cvt_pk_bf16_f32 v198, v182, v183\n" \
  "v_cvt_pk_bf16_f32 v199, v184, v185\n" \
  "v_cvt_pk_bf16_f32 v200, v186, v187\n" \
  "v_cvt_pk_bf16_f32 v201, v188, v189\n" \
  "v_cvt_pk_bf16_f32 v202, v190, v191\n" \
  "v_cvt_pk_bf16_f32 v203, v192, v193\n" \
  "v_cvt_pk_bf16_f32 v204, v194, v195\n" \
  "v_cvt_pk_bf16_f32 v205, v196, v197\n" \
  "v_exp_f32 v182, v166\n" \
  "v_exp_f32 v183, v167\n" \
  "v_exp_f32 v184, v168\n" \
  "v_mfma_f32_32x32x16_bf16 %0, v[198:201], v[238:241], %0\n" \
  "v_exp_f32 v185, v169\n" \
  "v_exp_f32 v186, v170\n" \
  "v_exp_f32 v187, v171\n" \
  "v_mfma_f32_32x32x16_bf16 %0, v[202:205], v[242:245], %0\n" \
  "v_exp_f32 v188, v172\n" \
  "v_exp_f32 v189, v173\n" \
  "v_exp_f32 v190, v174\n" \
  "v_mfma_f32_32x32x16_bf16 v[150:165], v[246:249], %10, 0\n" \
  "v_exp_f32 v191, v175\n" \
  "v_exp_f32 v192, v176\n" \
  "v_exp_f32 v193, v177\n" \
  "s_waitcnt lgkmcnt(7)\n" \
  "v_mfma_f32_32x32x16_bf16 v[150:165], %6, v[222:225], v[150:165]\n" \
  "v_exp_f32 v194, v178\n" \
  "v_exp_f32 v195, v179\n" \
  "v_exp_f32 v196, v180\n" \
  "v_exp_f32 v197, v181\n" \
  "v_pk_fma_f32 v[182:183], v[182:183], s[66:67], s[66:67]\n" \
  "v_pk_fma_f32 v[184:185], v[184:185], s[66:67], s[66:67]\n" \
  "v_pk_fma_f32 v[186:187], v[186:187], s[66:67], s[66:67]\n" \
  "v_pk_fma_f32 v[188:189], v[188:189], s[66:67], s[66:67]\n" \
  "v_pk_fma_f32 v[190:191], v[190:191], s[66:67], s[66:67]\n" \
  "v_pk_fma_f32 v[192:193], v[192:193], s[66:67], s[66:67]\n" \
  "v_pk_fma_f32 v[194:195], v[194:195], s[66:67], s[66:67]\n" \
  "v_pk_fma_f32 v[196:197], v[196:197], s[66:67], s[66:67]\n" \
  "s_waitcnt lgkmcnt(5)\n" \
  "v_mfma_f32_32x32x16_bf16 v[150:165], %7, v[226:229], v[150:165]\n" \
  "v_rcp_f32 v182, v182\n" \
  "v_rcp_f32 v183, v183\n" \
  "v_rcp_f32 v184, v184\n" \
  "v_rcp_f32 v185, v185\n" \
  "v_rcp_f32 v186, v186\n" \
  "v_rcp_f32 v187, v187\n" \
  "v_rcp_f32 v188, v188\n" \
  "v_rcp_f32 v189, v189\n" \
  "v_rcp_f32 v190, v190\n" \
  "v_rcp_f32 v191, v191\n" \
  "v_rcp_f32 v192, v192\n" \
  "v_rcp_f32 v193, v193\n" \
  "v_rcp_f32 v194, v194\n" \
  "v_rcp_f32 v195, v195\n" \
  "v_rcp_f32 v196, v196\n" \
  "v_rcp_f32 v197, v197\n" \
  "v_pk_mul_f32 v[182:183], v[182:183], v[166:167]\n" \
  "v_pk_mul_f32 v[184:185], v[184:185], v[168:169]\n" \
  "v_pk_mul_f32 v[186:187], v[186:187], v[170:171]\n" \
  "v_pk_mul_f32 v[188:189], v[188:189], v[172:173]\n" \
  "v_pk_mul_f32 v[190:191], v[190:191], v[174:175]\n" \
  "v_pk_mul_f32 v[192:193], v[192:193], v[176:177]\n" \
  "v_pk_mul_f32 v[194:195], v[194:195], v[178:179]\n" \
  "v_pk_mul_f32 v[196:197], v[196:197], v[180:181]\n" \
  "v_cvt_pk_bf16_f32 v198, v182, v183\n" \
  "v_cvt_pk_bf16_f32 v199, v184, v185\n" \
  "v_cvt_pk_bf16_f32 v200, v186, v187\n" \
  "v_cvt_pk_bf16_f32 v201, v188, v189\n" \
  "v_cvt_pk_bf16_f32 v202, v190, v191\n" \
  "v_cvt_pk_bf16_f32 v203, v192, v193\n" \
  "v_cvt_pk_bf16_f32 v204, v194, v195\n" \
  "v_cvt_pk_bf16_f32 v205, v196, v197\n" \
  "v_exp_f32 v182, v150\n" \
  "v_exp_f32 v183, v151\n" \
  "v_exp_f32 v184, v152\n" \
  "v_mfma_f32_32x32x16_bf16 %1, v[198:201], v[238:241], %1\n" \
  "v_exp_f32 v185, v153\n" \
  "v_exp_f32 v186, v154\n" \
  "v_exp_f32 v187, v155\n" \
  "v_mfma_f32_32x32x16_bf16 %1, v[202:205], v[242:245], %1\n" \
  "v_exp_f32 v188, v156\n" \
  "v_exp_f32 v189, v157\n" \
  "v_exp_f32 v190, v158\n" \
  "v_mfma_f32_32x32x16_bf16 v[166:181], v[246:249], %11, 0\n" \
  "v_exp_f32 v191, v159\n" \
  "v_exp_f32 v192, v160\n" \
  "v_exp_f32 v193, v161\n" \
  "s_waitcnt lgkmcnt(3)\n" \
  "v_mfma_f32_32x32x16_bf16 v[166:181], %6, v[230:233], v[166:181]\n" \
  "v_exp_f32 v194, v162\n" \
  "v_exp_f32 v195, v163\n" \
  "v_exp_f32 v196, v164\n" \
  "v_exp_f32 v197, v165\n" \
  "v_pk_fma_f32 v[182:183], v[182:183], s[66:67], s[66:67]\n" \
  "v_pk_fma_f32 v[184:185], v[184:185], s[66:67], s[66:67]\n" \
  "v_pk_fma_f32 v[186:187], v[186:187], s[66:67], s[66:67]\n" \
  "v_pk_fma_f32 v[188:189], v[188:189], s[66:67], s[66:67]\n" \
  "v_pk_fma_f32 v[190:191], v[190:191], s[66:67], s[66:67]\n" \
  "v_pk_fma_f32 v[192:193], v[192:193], s[66:67], s[66:67]\n" \
  "v_pk_fma_f32 v[194:195], v[194:195], s[66:67], s[66:67]\n" \
  "v_pk_fma_f32 v[196:197], v[196:197], s[66:67], s[66:67]\n" \
  "s_waitcnt lgkmcnt(1)\n" \
  "v_mfma_f32_32x32x16_bf16 v[166:181], %7, v[234:237], v[166:181]\n" \
  "v_rcp_f32 v182, v182\n" \
  "v_rcp_f32 v183, v183\n" \
  "v_rcp_f32 v184, v184\n" \
  "v_rcp_f32 v185, v185\n" \
  "v_rcp_f32 v186, v186\n" \
  "v_rcp_f32 v187, v187\n" \
  "v_rcp_f32 v188, v188\n" \
  "v_rcp_f32 v189, v189\n" \
  "v_rcp_f32 v190, v190\n" \
  "v_rcp_f32 v191, v191\n" \
  "v_rcp_f32 v192, v192\n" \
  "v_rcp_f32 v193, v193\n" \
  "v_rcp_f32 v194, v194\n" \
  "v_rcp_f32 v195, v195\n" \
  "v_rcp_f32 v196, v196\n" \
  "v_rcp_f32 v197, v197\n" \
  "v_pk_mul_f32 v[182:183], v[182:183], v[150:151]\n" \
  "v_pk_mul_f32 v[184:185], v[184:185], v[152:153]\n" \
  "v_pk_mul_f32 v[186:187], v[186:187], v[154:155]\n" \
  "v_pk_mul_f32 v[188:189], v[188:189], v[156:157]\n" \
  "v_pk_mul_f32 v[190:191], v[190:191], v[158:159]\n" \
  "v_pk_mul_f32 v[192:193], v[192:193], v[160:161]\n" \
  "v_pk_mul_f32 v[194:195], v[194:195], v[162:163]\n" \
  "v_pk_mul_f32 v[196:197], v[196:197], v[164:165]\n" \
  "v_cvt_pk_bf16_f32 v198, v182, v183\n" \
  "v_cvt_pk_bf16_f32 v199, v184, v185\n" \
  "v_cvt_pk_bf16_f32 v200, v186, v187\n" \
  "v_cvt_pk_bf16_f32 v201, v188, v189\n" \
  "v_cvt_pk_bf16_f32 v202, v190, v191\n" \
  "v_cvt_pk_bf16_f32 v203, v192, v193\n" \
  "v_cvt_pk_bf16_f32 v204, v194, v195\n" \
  "v_cvt_pk_bf16_f32 v205, v196, v197\n" \
  "v_exp_f32 v182, v166\n" \
  "v_exp_f32 v183, v167\n" \
  "v_exp_f32 v184, v168\n" \
  "v_mfma_f32_32x32x16_bf16 %2, v[198:201], v[238:241], %2\n" \
  "v_exp_f32 v185, v169\n" \
  "v_exp_f32 v186, v170\n" \
  "v_exp_f32 v187, v171\n" \
  "v_mfma_f32_32x32x16_bf16 %2, v[202:205], v[242:245], %2\n" \
  "v_exp_f32 v188, v172\n" \
  "v_exp_f32 v189, v173\n" \
  "v_exp_f32 v190, v174\n" \
  "v_exp_f32 v191, v175\n" \
  "v_exp_f32 v192, v176\n" \
  "v_exp_f32 v193, v177\n" \
  "v_exp_f32 v194, v178\n" \
  "v_exp_f32 v195, v179\n" \
  "v_exp_f32 v196, v180\n" \
  "v_exp_f32 v197, v181\n" \
  "v_pk_fma_f32 v[182:183], v[182:183], s[66:67], s[66:67]\n" \
  "v_pk_fma_f32 v[184:185], v[184:185], s[66:67], s[66:67]\n" \
  "v_pk_fma_f32 v[186:187], v[186:187], s[66:67], s[66:67]\n" \
  "v_pk_fma_f32 v[188:189], v[188:189], s[66:67], s[66:67]\n" \
  "v_pk_fma_f32 v[190:191], v[190:191], s[66:67], s[66:67]\n" \
  "v_pk_fma_f32 v[192:193], v[192:193], s[66:67], s[66:67]\n" \
  "v_pk_fma_f32 v[194:195], v[194:195], s[66:67], s[66:67]\n" \
  "v_pk_fma_f32 v[196:197], v[196:197], s[66:67], s[66:67]\n" \
  "v_rcp_f32 v182, v182\n" \
  "v_rcp_f32 v183, v183\n" \
  "v_rcp_f32 v184, v184\n" \
  "v_rcp_f32 v185, v185\n" \
  "v_rcp_f32 v186, v186\n" \
  "v_rcp_f32 v187, v187\n" \
  "v_rcp_f32 v188, v188\n" \
  "v_rcp_f32 v189, v189\n" \
  "v_rcp_f32 v190, v190\n" \
  "v_rcp_f32 v191, v191\n" \
  "v_rcp_f32 v192, v192\n" \
  "v_rcp_f32 v193, v193\n" \
  "v_rcp_f32 v194, v194\n" \
  "v_rcp_f32 v195, v195\n" \
  "v_rcp_f32 v196, v196\n" \
  "v_rcp_f32 v197, v197\n" \
  "v_pk_mul_f32 v[182:183], v[182:183], v[166:167]\n" \
  "v_pk_mul_f32 v[184:185], v[184:185], v[168:169]\n" \
  "v_pk_mul_f32 v[186:187], v[186:187], v[170:171]\n" \
  "v_pk_mul_f32 v[188:189], v[188:189], v[172:173]\n" \
  "v_pk_mul_f32 v[190:191], v[190:191], v[174:175]\n" \
  "v_pk_mul_f32 v[192:193], v[192:193], v[176:177]\n" \
  "v_pk_mul_f32 v[194:195], v[194:195], v[178:179]\n" \
  "v_pk_mul_f32 v[196:197], v[196:197], v[180:181]\n" \
  "v_cvt_pk_bf16_f32 v198, v182, v183\n" \
  "v_cvt_pk_bf16_f32 v199, v184, v185\n" \
  "v_cvt_pk_bf16_f32 v200, v186, v187\n" \
  "v_cvt_pk_bf16_f32 v201, v188, v189\n" \
  "v_cvt_pk_bf16_f32 v202, v190, v191\n" \
  "v_cvt_pk_bf16_f32 v203, v192, v193\n" \
  "v_cvt_pk_bf16_f32 v204, v194, v195\n" \
  "v_cvt_pk_bf16_f32 v205, v196, v197\n" \
  "s_setprio 3\n" \
  "s_nop 0\n" \
  "v_mfma_f32_32x32x16_bf16 %3, v[198:201], v[238:241], %3\n" \
  "v_mfma_f32_32x32x16_bf16 %3, v[202:205], v[242:245], %3\n" \
  "s_branch .Lend_%=\n" \
  ".Lv3_%=:\n" \
  "s_cmp_gt_i32 s60, 16\n" \
  "s_cbranch_scc0 .Lv2_%=\n" \
  "v_exp_f32 v182, v150\n" \
  "v_mov_b32 v204, 0\n" \
  "v_mov_b32 v205, 0\n" \
  "v_exp_f32 v183, v151\n" \
  "v_mfma_f32_32x32x16_bf16 v[166:181], v[246:249], %9, 0\n" \
  "v_exp_f32 v184, v152\n" \
  "v_exp_f32 v185, v153\n" \
  "v_exp_f32 v186, v154\n" \
  "s_waitcnt lgkmcnt(11)\n" \
  "v_mfma_f32_32x32x16_bf16 v[166:181], %6, v[214:217], v[166:181]\n" \
  "v_exp_f32 v187, v155\n" \
  "v_exp_f32 v188, v156\n" \
  "v_exp_f32 v189, v157\n" \
  "s_waitcnt lgkmcnt(9)\n" \
  "v_mfma_f32_32x32x16_bf16 v[166:181], %7, v[218:221], v[166:181]\n" \
  "v_exp_f32 v190, v158\n" \
  "v_exp_f32 v191, v159\n" \
  "v_exp_f32 v192, v160\n" \
  "v_exp_f32 v193, v161\n" \
  "v_pk_fma_f32 v[182:183], v[182:183], s[66:67], s[66:67]\n" \
  "v_pk_fma_f32 v[184:185], v[184:185], s[66:67], s[66:67]\n" \
  "v_pk_fma_f32 v[186:187], v[186:187], s[66:67], s[66:67]\n" \
  "v_pk_fma_f32 v[188:189], v[188:189], s[66:67], s[66:67]\n" \
  "v_pk_fma_f32 v[190:191], v[190:191], s[66:67], s[66:67]\n" \
  "v_pk_fma_f32 v[192:193], v[192:193], s[66:67], s[66:67]\n" \
  "v_rcp_f32 v182, v182\n" \
  "v_rcp_f32 v183, v183\n" \
  "v_rcp_f32 v184, v184\n" \
  "v_rcp_f32 v185, v185\n" \
  "v_rcp_f32 v186, v186\n" \
  "v_rcp_f32 v187, v187\n" \
  "v_rcp_f32 v188, v188\n" \
  "v_rcp_f32 v189, v189\n" \
  "v_rcp_f32 v190, v190\n" \
  "v_rcp_f32 v191, v191\n" \
  "v_rcp_f32 v192, v192\n" \
  "v_rcp_f32 v193, v193\n" \
  "v_pk_mul_f32 v[182:183], v[182:183], v[150:151]\n" \
  "v_pk_mul_f32 v[184:185], v[184:185], v[152:153]\n" \
  "v_pk_mul_f32 v[186:187], v[186:187], v[154:155]\n" \
  "v_pk_mul_f32 v[188:189], v[188:189], v[156:157]\n" \
  "v_pk_mul_f32 v[190:191], v[190:191], v[158:159]\n" \
  "v_pk_mul_f32 v[192:193], v[192:193], v[160:161]\n" \
  "v_cvt_pk_bf16_f32 v198, v182, v183\n" \
  "v_cvt_pk_bf16_f32 v199, v184, v185\n" \
  "v_cvt_pk_bf16_f32 v200, v186, v187\n" \
  "v_cvt_pk_bf16_f32 v201, v188, v189\n" \
  "v_cvt_pk_bf16_f32 v202, v190, v191\n" \
  "v_cvt_pk_bf16_f32 v203, v192, v193\n" \
  "v_exp_f32 v182, v166\n" \
  "v_exp_f32 v183, v167\n" \
  "v_mfma_f32_32x32x16_bf16 %0, v[198:201], v[238:241], %0\n" \
  "v_exp_f32 v184, v168\n" \
  "v_exp_f32 v185, v169\n" \
  "v_exp_f32 v186, v170\n" \
  "v_mfma_f32_32x32x16_bf16 %0, v[202:205], v[242:245], %0\n" \
  "v_exp_f32 v187, v171\n" \
  "v_exp_f32 v188, v172\n" \
  "v_exp_f32 v189, v173\n" \
  "v_mfma_f32_32x32x16_bf16 v[150:165], v[246:249], %10, 0\n" \
  "v_exp_f32 v190, v174\n" \
  "v_exp_f32 v191, v175\n" \
  "v_exp_f32 v192, v176\n" \
  "v_exp_f32 v193, v177\n" \
  "v_pk_fma_f32 v[182:183], v[182:183], s[66:67], s[66:67]\n" \
  "v_pk_fma_f32 v[184:185], v[184:185], s[66:67], s[66:67]\n" \
  "v_pk_fma_f32 v[186:187], v[186:187], s[66:67], s[66:67]\n" \
  "v_pk_fma_f32 v[188:189], v[188:189], s[66:67], s[66:67]\n" \
  "v_pk_fma_f32 v[190:191], v[190:191], s[66:67], s[66:67]\n" \
  "v_pk_fma_f32 v[192:193], v[192:193], s[66:67], s[66:67]\n" \
  "s_waitcnt lgkmcnt(7)\n" \
  "v_mfma_f32_32x32x16_bf16 v[150:165], %6, v[222:225], v[150:165]\n" \
  "v_rcp_f32 v182, v182\n" \
  "v_rcp_f32 v183, v183\n" \
  "v_rcp_f32 v184, v184\n" \
  "s_waitcnt lgkmcnt(5)\n" \
  "v_mfma_f32_32x32x16_bf16 v[150:165], %7, v[226:229], v[150:165]\n" \
  "v_rcp_f32 v185, v185\n" \
  "v_rcp_f32 v186, v186\n" \
  "v_rcp_f32 v187, v187\n" \
  "v_rcp_f32 v188, v188\n" \
  "v_rcp_f32 v189, v189\n" \
  "v_rcp_f32 v190, v190\n" \
  "v_rcp_f32 v191, v191\n" \
  "v_rcp_f32 v192, v192\n" \
  "v_rcp_f32 v193, v193\n" \
  "v_pk_mul_f32 v[182:183], v[182:183], v[166:167]\n" \
  "v_pk_mul_f32 v[184:185], v[184:185], v[168:169]\n" \
  "v_pk_mul_f32 v[186:187], v[186:187], v[170:171]\n" \
  "v_pk_mul_f32 v[188:189], v[188:189], v[172:173]\n" \
  "v_pk_mul_f32 v[190:191], v[190:191], v[174:175]\n" \
  "v_pk_mul_f32 v[192:193], v[192:193], v[176:177]\n" \
  "v_cvt_pk_bf16_f32 v198, v182, v183\n" \
  "v_cvt_pk_bf16_f32 v199, v184, v185\n" \
  "v_cvt_pk_bf16_f32 v200, v186, v187\n" \
  "v_cvt_pk_bf16_f32 v201, v188, v189\n" \
  "v_cvt_pk_bf16_f32 v202, v190, v191\n" \
  "v_cvt_pk_bf16_f32 v203, v192, v193\n" \
  "v_exp_f32 v182, v150\n" \
  "v_exp_f32 v183, v151\n" \
  "v_mfma_f32_32x32x16_bf16 %1, v[198:201], v[238:241], %1\n" \
  "v_exp_f32 v184, v152\n" \
  "v_exp_f32 v185, v153\n" \
  "v_exp_f32 v186, v154\n" \
  "v_mfma_f32_32x32x16_bf16 %1, v[202:205], v[242:245], %1\n" \
  "v_exp_f32 v187, v155\n" \
  "v_exp_f32 v188, v156\n" \
  "v_exp_f32 v189, v157\n" \
  "v_mfma_f32_32x32x16_bf16 v[166:181], v[246:249], %11, 0\n" \
  "v_exp_f32 v190, v158\n" \
  "v_exp_f32 v191, v159\n" \
  "v_exp_f32 v192, v160\n" \
  "v_exp_f32 v193, v161\n" \
  "v_pk_fma_f32 v[182:183], v[182:183], s[66:67], s[66:67]\n" \
  "v_pk_fma_f32 v[184:185], v[184:185], s[66:67], s[66:67]\n" \
  "v_pk_fma_f32 v[186:187], v[186:187], s[66:67], s[66:67]\n" \
  "v_pk_fma_f32 v[188:189], v[188:189], s[66:67], s[66:67]\n" \
  "v_pk_fma_f32 v[190:191], v[190:191], s[66:67], s[66:67]\n" \
  "v_pk_fma_f32 v[192:193], v[192:193], s[66:67], s[66:67]\n" \
  "s_waitcnt lgkmcnt(3)\n" \
  "v_mfma_f32_32x32x16_bf16 v[166:181], %6, v[230:233], v[166:181]\n" \
  "v_rcp_f32 v182, v182\n" \
  "v_rcp_f32 v183, v183\n" \
  "v_rcp_f32 v184, v184\n" \
  "s_waitcnt lgkmcnt(1)\n" \
  "v_mfma_f32_32x32x16_bf16 v[166:181], %7, v[234:237], v[166:181]\n" \
  "v_rcp_f32 v185, v185\n" \
  "v_rcp_f32 v186, v186\n" \
  "v_rcp_f32 v187, v187\n" \
  "v_rcp_f32 v188, v188\n" \
  "v_rcp_f32 v189, v189\n" \
  "v_rcp_f32 v190, v190\n" \
  "v_rcp_f32 v191, v191\n" \
  "v_rcp_f32 v192, v192\n" \
  "v_rcp_f32 v193, v193\n" \
  "v_pk_mul_f32 v[182:183], v[182:183], v[150:151]\n" \
  "v_pk_mul_f32 v[184:185], v[184:185], v[152:153]\n" \
  "v_pk_mul_f32 v[186:187], v[186:187], v[154:155]\n" \
  "v_pk_mul_f32 v[188:189], v[188:189], v[156:157]\n" \
  "v_pk_mul_f32 v[190:191], v[190:191], v[158:159]\n" \
  "v_pk_mul_f32 v[192:193], v[192:193], v[160:161]\n" \
  "v_cvt_pk_bf16_f32 v198, v182, v183\n" \
  "v_cvt_pk_bf16_f32 v199, v184, v185\n" \
  "v_cvt_pk_bf16_f32 v200, v186, v187\n" \
  "v_cvt_pk_bf16_f32 v201, v188, v189\n" \
  "v_cvt_pk_bf16_f32 v202, v190, v191\n" \
  "v_cvt_pk_bf16_f32 v203, v192, v193\n" \
  "v_exp_f32 v182, v166\n" \
  "v_exp_f32 v183, v167\n" \
  "v_mfma_f32_32x32x16_bf16 %2, v[198:201], v[238:241], %2\n" \
  "v_exp_f32 v184, v168\n" \
  "v_exp_f32 v185, v169\n" \
  "v_exp_f32 v186, v170\n" \
  "v_mfma_f32_32x32x16_bf16 %2, v[202:205], v[242:245], %2\n" \
  "v_exp_f32 v187, v171\n" \
  "v_exp_f32 v188, v172\n" \
  "v_exp_f32 v189, v173\n" \
  "v_exp_f32 v190, v174\n" \
  "v_exp_f32 v191, v175\n" \
  "v_exp_f32 v192, v176\n" \
  "v_exp_f32 v193, v177\n" \
  "v_pk_fma_f32 v[182:183], v[182:183], s[66:67], s[66:67]\n" \
  "v_pk_fma_f32 v[184:185], v[184:185], s[66:67], s[66:67]\n" \
  "v_pk_fma_f32 v[186:187], v[186:187], s[66:67], s[66:67]\n" \
  "v_pk_fma_f32 v[188:189], v[188:189], s[66:67], s[66:67]\n" \
  "v_pk_fma_f32 v[190:191], v[190:191], s[66:67], s[66:67]\n" \
  "v_pk_fma_f32 v[192:193], v[192:193], s[66:67], s[66:67]\n" \
  "v_rcp_f32 v182, v182\n" \
  "v_rcp_f32 v183, v183\n" \
  "v_rcp_f32 v184, v184\n" \
  "v_rcp_f32 v185, v185\n" \
  "v_rcp_f32 v186, v186\n" \
  "v_rcp_f32 v187, v187\n" \
  "v_rcp_f32 v188, v188\n" \
  "v_rcp_f32 v189, v189\n" \
  "v_rcp_f32 v190, v190\n" \
  "v_rcp_f32 v191, v191\n" \
  "v_rcp_f32 v192, v192\n" \
  "v_rcp_f32 v193, v193\n" \
  "v_pk_mul_f32 v[182:183], v[182:183], v[166:167]\n" \
  "v_pk_mul_f32 v[184:185], v[184:185], v[168:169]\n" \
  "v_pk_mul_f32 v[186:187], v[186:187], v[170:171]\n" \
  "v_pk_mul_f32 v[188:189], v[188:189], v[172:173]\n" \
  "v_pk_mul_f32 v[190:191], v[190:191], v[174:175]\n" \
  "v_pk_mul_f32 v[192:193], v[192:193], v[176:177]\n" \
  "v_cvt_pk_bf16_f32 v198, v182, v183\n" \
  "v_cvt_pk_bf16_f32 v199, v184, v185\n" \
  "v_cvt_pk_bf16_f32 v200, v186, v187\n" \
  "v_cvt_pk_bf16_f32 v201, v188, v189\n" \
  "v_cvt_pk_bf16_f32 v202, v190, v191\n" \
  "v_cvt_pk_bf16_f32 v203, v192, v193\n" \
  "s_setprio 3\n" \
  "s_nop 0\n" \
  "v_mfma_f32_32x32x16_bf16 %3, v[198:201], v[238:241], %3\n" \
  "v_mfma_f32_32x32x16_bf16 %3, v[202:205], v[242:245], %3\n" \
  "s_branch .Lend_%=\n" \
  ".Lv2_%=:\n" \
  "s_cmp_gt_i32 s60, 8\n" \
  "s_cbranch_scc0 .Lv1_%=\n" \
  "v_exp_f32 v182, v150\n" \
  "v_exp_f32 v183, v151\n" \
  "v_mfma_f32_32x32x16_bf16 v[166:181], v[246:249], %9, 0\n" \
  "v_exp_f32 v184, v152\n" \
  "v_exp_f32 v185, v153\n" \
  "s_waitcnt lgkmcnt(11)\n" \
  "v_mfma_f32_32x32x16_bf16 v[166:181], %6, v[214:217], v[166:181]\n" \
  "v_exp_f32 v186, v154\n" \
  "v_exp_f32 v187, v155\n" \
  "v_exp_f32 v188, v156\n" \
  "v_exp_f32 v189, v157\n" \
  "v_pk_fma_f32 v[182:183], v[182:183], s[66:67], s[66:67]\n" \
  "v_pk_fma_f32 v[184:185], v[184:185], s[66:67], s[66:67]\n" \
  "v_pk_fma_f32 v[186:187], v[186:187], s[66:67], s[66:67]\n" \
  "v_pk_fma_f32 v[188:189], v[188:189], s[66:67], s[66:67]\n" \
  "v_rcp_f32 v182, v182\n" \
  "v_rcp_f32 v183, v183\n" \
  "v_rcp_f32 v184, v184\n" \
  "v_rcp_f32 v185, v185\n" \
  "v_rcp_f32 v186, v186\n" \
  "v_rcp_f32 v187, v187\n" \
  "v_rcp_f32 v188, v188\n" \
  "v_rcp_f32 v189, v189\n" \
  "v_pk_mul_f32 v[182:183], v[182:183], v[150:151]\n" \
  "v_pk_mul_f32 v[184:185], v[184:185], v[152:153]\n" \
  "v_pk_mul_f32 v[186:187], v[186:187], v[154:155]\n" \
  "v_pk_mul_f32 v[188:189], v[188:189], v[156:157]\n" \
  "v_cvt_pk_bf16_f32 v198, v182, v183\n" \
  "v_cvt_pk_bf16_f32 v199, v184, v185\n" \
  "v_cvt_pk_bf16_f32 v200, v186, v187\n" \
  "v_cvt_pk_bf16_f32 v201, v188, v189\n" \
  "v_exp_f32 v182, v166\n" \
  "v_exp_f32 v183, v167\n" \
  "v_mfma_f32_32x32x16_bf16 %0, v[198:201], v[238:241], %0\n" \
  "v_exp_f32 v184, v168\n" \
  "v_exp_f32 v185, v169\n" \
  "v_mfma_f32_32x32x16_bf16 v[150:165], v[246:249], %10, 0\n" \
  "v_exp_f32 v186, v170\n" \
  "v_exp_f32 v187, v171\n" \
  "v_exp_f32 v188, v172\n" \
  "v_exp_f32 v189, v173\n" \
  "v_pk_fma_f32 v[182:183], v[182:183], s[66:67], s[66:67]\n" \
  "v_pk_fma_f32 v[184:185], v[184:185], s[66:67], s[66:67]\n" \
  "v_pk_fma_f32 v[186:187], v[186:187], s[66:67], s[66:67]\n" \
  "v_pk_fma_f32 v[188:189], v[188:189], s[66:67], s[66:67]\n" \
  "s_waitcnt lgkmcnt(7)\n" \
  "v_mfma_f32_32x32x16_bf16 v[150:165], %6, v[222:225], v[150:165]\n" \
  "v_rcp_f32 v182, v182\n" \
  "v_rcp_f32 v183, v183\n" \
  "v_rcp_f32 v184, v184\n" \
  "v_rcp_f32 v185, v185\n" \
  "v_rcp_f32 v186, v186\n" \
  "v_rcp_f32 v187, v187\n" \
  "v_rcp_f32 v188, v188\n" \
  "v_rcp_f32 v189, v189\n" \
  "v_pk_mul_f32 v[182:183], v[182:183], v[166:167]\n" \
  "v_pk_mul_f32 v[184:185], v[184:185], v[168:169]\n" \
  "v_pk_mul_f32 v[186:187], v[186:187], v[170:171]\n" \
  "v_pk_mul_f32 v[188:189], v[188:189], v[172:173]\n" \
  "v_cvt_pk_bf16_f32 v198, v182, v183\n" \
  "v_cvt_pk_bf16_f32 v199, v184, v185\n" \
  "v_cvt_pk_bf16_f32 v200, v186, v187\n" \
  "v_cvt_pk_bf16_f32 v201, v188, v189\n" \
  "v_exp_f32 v182, v150\n" \
  "v_exp_f32 v183, v151\n" \
  "v_mfma_f32_32x32x16_bf16 %1, v[198:201], v[238:241], %1\n" \
  "v_exp_f32 v184, v152\n" \
  "v_exp_f32 v185, v153\n" \
  "v_mfma_f32_32x32x16_bf16 v[166:181], v[246:249], %11, 0\n" \
  "v_exp_f32 v186, v154\n" \
  "v_exp_f32 v187, v155\n" \
  "v_exp_f32 v188, v156\n" \
  "v_exp_f32 v189, v157\n" \
  "v_pk_fma_f32 v[182:183], v[182:183], s[66:67], s[66:67]\n" \
  "v_pk_fma_f32 v[184:185], v[184:185], s[66:67], s[66:67]\n" \
  "v_pk_fma_f32 v[186:187], v[186:187], s[66:67], s[66:67]\n" \
  "v_pk_fma_f32 v[188:189], v[188:189], s[66:67], s[66:67]\n" \
  "s_waitcnt lgkmcnt(3)\n" \
  "v_mfma_f32_32x32x16_bf16 v[166:181], %6, v[230:233], v[166:181]\n" \
  "v_rcp_f32 v182, v182\n" \
  "v_rcp_f32 v183, v183\n" \
  "v_rcp_f32 v184, v184\n" \
  "v_rcp_f32 v185, v185\n" \
  "v_rcp_f32 v186, v186\n" \
  "v_rcp_f32 v187, v187\n" \
  "v_rcp_f32 v188, v188\n" \
  "v_rcp_f32 v189, v189\n" \
  "v_pk_mul_f32 v[182:183], v[182:183], v[150:151]\n" \
  "v_pk_mul_f32 v[184:185], v[184:185], v[152:153]\n" \
  "v_pk_mul_f32 v[186:187], v[186:187], v[154:155]\n" \
  "v_pk_mul_f32 v[188:189], v[188:189], v[156:157]\n" \
  "v_cvt_pk_bf16_f32 v198, v182, v183\n" \
  "v_cvt_pk_bf16_f32 v199, v184, v185\n" \
  "v_cvt_pk_bf16_f32 v200, v186, v187\n" \
  "v_cvt_pk_bf16_f32 v201, v188, v189\n" \
  "v_exp_f32 v182, v166\n" \
  "v_exp_f32 v183, v167\n" \
  "v_mfma_f32_32x32x16_bf16 %2, v[198:201], v[238:241], %2\n" \
  "v_exp_f32 v184, v168\n" \
  "v_exp_f32 v185, v169\n" \
  "v_exp_f32 v186, v170\n" \
  "v_exp_f32 v187, v171\n" \
  "v_exp_f32 v188, v172\n" \
  "v_exp_f32 v189, v173\n" \
  "v_pk_fma_f32 v[182:183], v[182:183], s[66:67], s[66:67]\n" \
  "v_pk_fma_f32 v[184:185], v[184:185], s[66:67], s[66:67]\n" \
  "v_pk_fma_f32 v[186:187], v[186:187], s[66:67], s[66:67]\n" \
  "v_pk_fma_f32 v[188:189], v[188:189], s[66:67], s[66:67]\n" \
  "v_rcp_f32 v182, v182\n" \
  "v_rcp_f32 v183, v183\n" \
  "v_rcp_f32 v184, v184\n" \
  "v_rcp_f32 v185, v185\n" \
  "v_rcp_f32 v186, v186\n" \
  "v_rcp_f32 v187, v187\n" \
  "v_rcp_f32 v188, v188\n" \
  "v_rcp_f32 v189, v189\n" \
  "v_pk_mul_f32 v[182:183], v[182:183], v[166:167]\n" \
  "v_pk_mul_f32 v[184:185], v[184:185], v[168:169]\n" \
  "v_pk_mul_f32 v[186:187], v[186:187], v[170:171]\n" \
  "v_pk_mul_f32 v[188:189], v[188:189], v[172:173]\n" \
  "v_cvt_pk_bf16_f32 v198, v182, v183\n" \
  "v_cvt_pk_bf16_f32 v199, v184, v185\n" \
  "v_cvt_pk_bf16_f32 v200, v186, v187\n" \
  "v_cvt_pk_bf16_f32 v201, v188, v189\n" \
  "s_setprio 3\n" \
  "s_nop 0\n" \
  "v_mfma_f32_32x32x16_bf16 %3, v[198:201], v[238:241], %3\n" \
  "s_branch .Lend_%=\n" \
  ".Lv1_%=:\n" \
  "v_mfma_f32_32x32x16_bf16 v[166:181], v[246:249], %9, 0\n" \
  "v_exp_f32 v182, v150\n" \
  "v_mov_b32 v200, 0\n" \
  "v_mov_b32 v201, 0\n" \
  "v_exp_f32 v183, v151\n" \
  "v_exp_f32 v184, v152\n" \
  "v_exp_f32 v185, v153\n" \
  "v_pk_fma_f32 v[182:183], v[182:183], s[66:67], s[66:67]\n" \
  "v_pk_fma_f32 v[184:185], v[184:185], s[66:67], s[66:67]\n" \
  "s_waitcnt lgkmcnt(11)\n" \
  "v_mfma_f32_32x32x16_bf16 v[166:181], %6, v[214:217], v[166:181]\n" \
  "v_rcp_f32 v182, v182\n" \
  "v_rcp_f32 v183, v183\n" \
  "v_rcp_f32 v184, v184\n" \
  "v_rcp_f32 v185, v185\n" \
  "v_pk_mul_f32 v[182:183], v[182:183], v[150:151]\n" \
  "v_pk_mul_f32 v[184:185], v[184:185], v[152:153]\n" \
  "v_cvt_pk_bf16_f32 v198, v182, v183\n" \
  "v_cvt_pk_bf16_f32 v199, v184, v185\n" \
  "s_nop 3\n" \
  "v_exp_f32 v182, v166\n" \
  "v_exp_f32 v183, v167\n" \
  "v_mfma_f32_32x32x16_bf16 %0, v[198:201], v[238:241], %0\n" \
  "v_exp_f32 v184, v168\n" \
  "v_exp_f32 v185, v169\n" \
  "v_pk_fma_f32 v[182:183], v[182:183], s[66:67], s[66:67]\n" \
  "v_pk_fma_f32 v[184:185], v[184:185], s[66:67], s[66:67]\n" \
  "v_mfma_f32_32x32x16_bf16 v[150:165], v[246:249], %10, 0\n" \
  "v_rcp_f32 v182, v182\n" \
  "v_rcp_f32 v183, v183\n" \
  "v_rcp_f32 v184, v184\n" \
  "v_rcp_f32 v185, v185\n" \
  "v_pk_mul_f32 v[182:183], v[182:183], v[166:167]\n" \
  "v_pk_mul_f32 v[184:185], v[184:185], v[168:169]\n" \
  "s_waitcnt lgkmcnt(7)\n" \
  "v_mfma_f32_32x32x16_bf16 v[150:165], %6, v[222:225], v[150:165]\n" \
  "v_cvt_pk_bf16_f32 v198, v182, v183\n" \
  "v_cvt_pk_bf16_f32 v199, v184, v185\n" \
  "s_nop 9\n" \
  "v_exp_f32 v182, v150\n" \
  "v_exp_f32 v183, v151\n" \
  "v_mfma_f32_32x32x16_bf16 %1, v[198:201], v[238:241], %1\n" \
  "v_exp_f32 v184, v152\n" \
  "v_exp_f32 v185, v153\n" \
  "v_pk_fma_f32 v[182:183], v[182:183], s[66:67], s[66:67]\n" \
  "v_pk_fma_f32 v[184:185], v[184:185], s[66:67], s[66:67]\n" \
  "v_mfma_f32_32x32x16_bf16 v[166:181], v[246:249], %11, 0\n" \
  "v_rcp_f32 v182, v182\n" \
  "v_rcp_f32 v183, v183\n" \
  "v_rcp_f32 v184, v184\n" \
  "v_rcp_f32 v185, v185\n" \
  "v_pk_mul_f32 v[182:183], v[182:183], v[150:151]\n" \
  "v_pk_mul_f32 v[184:185], v[184:185], v[152:153]\n" \
  "s_waitcnt lgkmcnt(3)\n" \
  "v_mfma_f32_32x32x16_bf16 v[166:181], %6, v[230:233], v[166:181]\n" \
  "v_cvt_pk_bf16_f32 v198, v182, v183\n" \
  "v_cvt_pk_bf16_f32 v199, v184, v185\n" \
  "s_nop 9\n" \
  "v_exp_f32 v182, v166\n" \
  "v_exp_f32 v183, v167\n" \
  "v_mfma_f32_32x32x16_bf16 %2, v[198:201], v[238:241], %2\n" \
  "v_exp_f32 v184, v168\n" \
  "v_exp_f32 v185, v169\n" \
  "v_pk_fma_f32 v[182:183], v[182:183], s[66:67], s[66:67]\n" \
  "v_pk_fma_f32 v[184:185], v[184:185], s[66:67], s[66:67]\n" \
  "v_rcp_f32 v182, v182\n" \
  "v_rcp_f32 v183, v183\n" \
  "v_rcp_f32 v184, v184\n" \
  "v_rcp_f32 v185, v185\n" \
  "v_pk_mul_f32 v[182:183], v[182:183], v[166:167]\n" \
  "v_pk_mul_f32 v[184:185], v[184:185], v[168:169]\n" \
  "v_cvt_pk_bf16_f32 v198, v182, v183\n" \
  "v_cvt_pk_bf16_f32 v199, v184, v185\n" \
  "s_setprio 3\n" \
  "s_nop 0\n" \
  "v_mfma_f32_32x32x16_bf16 %3, v[198:201], v[238:241], %3\n" \
  ".Lend_%=:\n" \
  "s_mov_b32 m0, s62\n" \
  "s_waitcnt vmcnt(0) lgkmcnt(0)\n" \
  "s_nop 10\n"
#define LOOP_CLOBBERS "v150", "v151", "v152", "v153", "v154", "v155", "v156", "v157", "v158", "v159", "v160", "v161", "v162", "v163", "v164", "v165", "v166", "v167", "v168", "v169", "v170", "v171", "v172", "v173", "v174", "v175", "v176", "v177", "v178", "v179", "v180", "v181", "v182", "v183", "v184", "v185", "v186", "v187", "v188", "v189", "v190", "v191", "v192", "v193", "v194", "v195", "v196", "v197", "v198", "v199", "v200", "v201", "v202", "v203", "v204", "v205", "v206", "v207", "v208", "v209", "v210", "v211", "v212", "v213", "v214", "v215", "v216", "v217", "v218", "v219", "v220", "v221", "v222", "v223", "v224", "v225", "v226", "v227", "v228", "v229", "v230", "v231", "v232", "v233", "v234", "v235", "v236", "v237", "v238", "v239", "v240", "v241", "v242", "v243", "v244", "v245", "v246", "v247", "v248", "v249", "v250", "v251", "v252", "v253", "v254", "v255", "s60", "s61", "s62", "s64", "s65", "s66", "s67", "scc", "memory"

DI u32 act1(float y) {
  const float r = __builtin_amdgcn_rcpf(__builtin_fmaf(__builtin_amdgcn_exp2f(y), CL2, CL2));
  return pk_bf16(y * r, 0.f);
}

__global__ __launch_bounds__(512, 2) void k_egnn(const u16* __restrict__ h, const u16* __restrict__ pos, const u16* __restrict__ ea,
                                                 const int* __restrict__ srcg, const int* __restrict__ dstg,
                                                 const u16* __restrict__ we, const u16* __restrict__ wx, const u16* __restrict__ wh,
                                                 char* ws, u16* __restrict__ out) {
  __shared__ __attribute__((aligned(16))) char lds[L_TOTAL];
  unsigned long long pc0;
  u32 code_bytes;
  asm volatile("s_getpc_b64 %0\n.Legnn_code_begin:\n\ts_mov_b32 %1, .Legnn_code_end-.Legnn_code_begin" : "=s"(pc0), "=s"(code_bytes));
  pc0 &= ~15ull;
  const int tid = threadIdx.x, lane = tid & 63;
  const int w = __builtin_amdgcn_readfirstlane(tid >> 6);
  const int b = blockIdx.x;
  const int n0 = node0_of(b), nn = node0_of(b + 1) - n0;
  const u32 ldsbase = (u32)(size_t)(LDSP char*)lds;
  const int l15 = lane & 15, g4 = lane >> 4;
  const int tq = l15 >> 2, tp = l15 & 3;
  if (l15 < 6 && g4 == 0) dma1((const void*)pc0, min((u32)((b >> 3) * 8 + w) * 96u + (u32)lane * 16u, code_bytes & ~15u), ldsbase + L_JUNK);
  __builtin_amdgcn_sched_barrier(0);
  asm volatile("" :: "s"(h), "s"(pos), "s"(ea), "s"(srcg), "s"(dstg), "s"(we), "s"(wx), "s"(wh), "s"(ws), "s"(out));

  u32 gen, tb0 = 0, tb1 = 0, tb2 = 0;
  u32* cnt = (u32*)(lds + L_CNT);
  const u32 dum = (u32)(L_DUM + lane * 8);
  const int yt = tid & 255, wq = w & 3;
  const bool gx = w >= 4;
  const u32 wv0 = (u32)((16 * wq + g4) * 256 + (l15 ^ (g4 << 1)) * 16), wv1 = wv0 ^ 128u;
  auto stage_w = [&](const u16* g, int area) {
    dma4(g, wv0, wv1, ldsbase + area + wq * 4096);
    dma4(g + 64 * 128, wv0, wv1, ldsbase + area + 16384 + wq * 4096);
  };
  auto warm = [&]() {
    const u16* g = wq < 2 ? wh : (wq == 2 ? we + 256 * 128 : wx);
    const u32 o2 = wq < 2 ? (u32)((b >> 3) * 2048 + wq * 1024 + lane * 16) : (u32)(lane & (wq == 2 ? 31 : 15)) * 16u;
    dma1(g, o2, ldsbase + L_JUNK);
  };
  u32 dd[3], ss[3];
  if (!gx) {
    const u32 e0 = (u32)(b * CHUNK + yt) * 4u, e2 = e0 + (yt < CHUNK - 512 ? 2048u : 0u);
    gld4(dd[0], dstg, e0); gld4(dd[1], dstg, e0 + 1024u); gld4(dd[2], dstg, e2);
    gld4(ss[0], srcg, e0); gld4(ss[1], srcg, e0 + 1024u); gld4(ss[2], srcg, e2);
    if (w == 0) gld4c(gen, ws + WS_GEN, 0u);
    warm();
    const u32x4 z = {0, 0, 0, 0};
#pragma unroll
    for (int i = 0; i < 4; ++i) lds_st<u32x4>(lds, L_IMG + yt * 16 + i * 4096, z);
    if (yt < 64) lds_st<u32>(lds, L_CNT + yt * 4, 0u);
  }
  wg_barrier();
  if (gx) {
    __builtin_amdgcn_s_setprio(2);
#pragma unroll
    for (int i = 0; i < 3; ++i) {
      const int row = 12 * wq + 4 * i + g4;
      dma1(h, (u32)(n0 + min(row, nn - 1)) * 256u + (u32)((l15 ^ (row & 15)) * 16), ldsbase + L_HT + (12 * wq + 4 * i) * 256);
    }
    stage_w(we + 128 * 128, L_ROWS);
    stage_w(we, L_S);
  } else {
    if (w == 0) VMWAIT(2); else VMWAIT(1);
    asm volatile("" : "+v"(dd[0]), "+v"(ss[0]), "+v"(dd[1]), "+v"(ss[1]), "+v"(dd[2]), "+v"(ss[2]) :: "memory");
    u32 ad[3], rc[3], kk[3];
#pragma unroll
    for (int i = 0; i < 3; ++i) {
      const u32 bin = (dd[i] * 16u) / 625u;
      rc[i] = (ss[i] << 8) | ((dd[i] - ((625u * bin + 15u) >> 4)) << 2) | ((u32)(yt + 256 * i) << 22);
      ad[i] = (i < 2 || yt < CHUNK - 512) ? L_IMG + bin * 4 : dum;
      kk[i] = atomicAdd((u32*)(lds + ad[i]), 1u);
      atomicAdd((u32*)(lds + ((i < 2 || yt < CHUNK - 512) ? ad[i] + 1024 : dum)), rc[i]);
    }
    if (yt >= CHUNK - 512) kk[2] = 99u;
#pragma unroll
    for (int i = 0; i < 3; ++i) lds_st<u32>(lds, kk[i] < CAP ? ad[i] + 2048 + kk[i] * 1024 : dum, rc[i]);
    if (yt >= CHUNK - 512) kk[2] = 0u;
    if (__builtin_expect(__any(max(max(kk[0], kk[1]), kk[2]) >= CAP), 0)) cnt[61] = 1;
    if (w == 0) {
      asm volatile("s_waitcnt vmcnt(1)" : "+v"(gen) :: "memory");
      lds_st<u32>(lds, tid == 0 ? L_CNT + 54 * 4 : dum, gen + 1u);
    }
  }
  asm volatile("s_waitcnt vmcnt(8) lgkmcnt(0)" ::: "memory");
  if (lane == 0) atomicAdd(&cnt[gx ? 57 : 58], 1u);
  const int part = tid & 3;
  while (lds_peek(lds, L_CNT + (gx ? 57 : 58) * 4) < 4u) __builtin_amdgcn_s_sleep(1);
  asm volatile("" ::: "memory");

  const int hba = L_HT + l15 * 256;
  auto hfrag = [&](int nt, int s) { return lds_ld<bf16x8>(lds, hba + nt * 4096 + (((4 * s + g4) ^ l15) * 16)); };
  auto scale2 = [](float x, float y) {
    float a, c;
    asm("v_mul_f32 %0, 0xbfb8aa3b, %1" : "=v"(a) : "v"(x));
    asm("v_mul_f32 %0, 0xbfb8aa3b, %1" : "=v"(c) : "v"(y));
    return pk_bf16(a, c);
  };
  u32 tag = 0;
  const int wrow = L_S + (8 * g4 + tq) * 256, wkey = (tq | ((g4 & 1) << 2)) << 1;
  if (gx) {
    const int wb = wrow + 32768 + (((4 * wq + tp) ^ wkey) * 16);
    bf16x8 wf[2][4];
#pragma unroll
    for (int s = 0; s < 4; ++s)
#pragma unroll
      for (int u = 0; u < 2; ++u) wf[u][s] = cat4(lds_tr(lds, wb + u * 8 + s * 8192), lds_tr(lds, wb + u * 8 + s * 8192 + 1024));
    f32x4 a[3], c[3];
#pragma unroll
    for (int nt = 0; nt < 3; ++nt) { a[nt] = f32x4{0.f, 0.f, 0.f, 0.f}; c[nt] = a[nt]; }
#pragma unroll
    for (int s = 0; s < 4; ++s)
#pragma unroll
      for (int nt = 0; nt < 3; ++nt) { const bf16x8 hb = hfrag(nt, s); a[nt] = MFMA16(wf[0][s], hb, a[nt]); c[nt] = MFMA16(wf[1][s], hb, c[nt]); }
    asm volatile("s_nop 7" : "+v"(a[0]), "+v"(a[1]), "+v"(a[2]), "+v"(c[0]), "+v"(c[1]), "+v"(c[2]));
#pragma unroll
    for (int nt = 0; nt < 3; ++nt) {
      const u32x4 o = {scale2(a[nt][0], a[nt][1]), scale2(a[nt][2], a[nt][3]), scale2(c[nt][0], c[nt][1]), scale2(c[nt][2], c[nt][3])};
      gst16c(ws + WS_BP, (u32)((n0 + min(16 * nt + l15, nn - 1)) * 256 + 64 * wq + 16 * g4), o);
    }
    VMWAIT(3);
    if (lane == 0) atomicAdd(&cnt[56], 1u);
  } else {
    tag = lds_peek(lds, L_CNT + 54 * 4);
    u32x4 v[4];
#pragma unroll
    for (int i = 0; i < 4; ++i)
#pragma unroll
      for (int j = 0; j < 4; ++j) v[i][j] = lds_ld<u32>(lds, L_IMG + part * 1024 + (yt >> 2) * 4 + j * 4096 + i * 256);
    if (__builtin_expect(__builtin_amdgcn_readfirstlane((int)cnt[61]), 0)) {
#pragma unroll
      for (int i = 0; i < 4; ++i)
        if (__builtin_amdgcn_update_dpp(0u, v[i][0], 0x00, 0xf, 0xf, false) > CAP) v[i] = u32x4{part ? 0u : v[i][0], 0, 0, 0};
    }
    const u32 hadd = part == 0 ? tag << 10 : (part == 1 ? tag : 0u);
    const u32 off = (u32)((yt >> 2) * 16384 + b * 64 + part * 16);
#pragma unroll
    for (int i = 0; i < 4; ++i) { v[i][0] += hadd; gst16c(ws + WS_SLOTS, off + (u32)i * 64u * 16384u, v[i]); }
    if (lane == 0) atomicAdd(&cnt[53], 1u);
  }
  const int r = lane & 31, hh = lane >> 5;
  const int rowbuf = L_ROWS + w * 8192;
  const int sbase = L_S + w * 2560;
  bf16x8 id[2];
  {
    const int dlt = r - 8 * hh;
    const u32 one = (dlt & 1) ? 0x3f800000u : 0x3f80u;
    u32x4 t0, t1;
#pragma unroll
    for (int i = 0; i < 4; ++i) { t0[i] = (dlt >> 1) == i ? one : 0u; t1[i] = (dlt >> 1) == 8 + i ? one : 0u; }
    id[0] = __builtin_bit_cast(bf16x8, t0); id[1] = __builtin_bit_cast(bf16x8, t1);
  }
  u32 boff0 = ldsbase + rowbuf + hh * 1024 + tq * 128 + (((2 * (g4 & 1) + (tp >> 1)) ^ ((tq >> 1) << 2)) * 16) + (tp & 1) * 8;
  u32 boff1 = boff0 ^ 64u;
  u32 soff = ldsbase + sbase + (4 * hh + tq) * 64 + (16 * (g4 & 1) + 4 * tp) * 2;
  u32 x2a = ldsbase + sbase + 2048 + r * 16;
  u32 swb = ldsbase + sbase + r * 64;
  u32 eoff = hh ? x2a : swb;
  u32 dc = (u32)(((lane & 7) ^ ((g4 & 1) << 2)) * 16);
  u32 zad = ldsbase + sbase + lane * 8;
  u32 wa = (u32)(wrow + 32768 + (((2 * w + (tp >> 1)) ^ wkey) * 16) + (tp & 1) * 8);
  asm volatile("" : "+v"(id[0]), "+v"(id[1]), "+v"(boff0), "+v"(boff1), "+v"(soff), "+v"(x2a), "+v"(swb), "+v"(eoff), "+v"(dc),
                    "+v"(zad), "+v"(wa));
  int passes = 0;
  u32x4 fl[2];
  if (gx) {
    const u32 qoff = (u32)(b * 16384 + yt * 16);
    while (lds_peek(lds, L_CNT + 58 * 4) < 4u) __builtin_amdgcn_s_sleep(1);
    tag = lds_peek(lds, L_CNT + 54 * 4);
    VMWAIT(0);
    if (lane == 0 && atomicAdd(&cnt[60], 1u) == 3u) { const u32x2 f = {tag, ~tag}; gst8c(ws + WS_FLAG, (u32)b * 8u, f); }
    u32x4 q[4];
    u32 sc[4];
#pragma clang loop unroll(disable)
    for (;;) {
      gld16c4w(q, ws + WS_SLOTS, qoff);
      u32 bad = 0;
#pragma unroll
      for (int i = 0; i < 4; ++i) {
        const u32 h0 = __builtin_amdgcn_update_dpp(0u, q[i][0], 0x00, 0xf, 0xf, true);
        const u32 h1 = __builtin_amdgcn_update_dpp(0u, q[i][0], 0x55, 0xf, 0xf, true);
        u32 s = q[i][1] + q[i][2] + q[i][3] + (part >= 2 ? q[i][0] : 0u);
        s += __builtin_amdgcn_update_dpp(0u, s, 0xB1, 0xf, 0xf, true);
        s += __builtin_amdgcn_update_dpp(0u, s, 0x4E, 0xf, 0xf, true);
        sc[i] = h0 - (tag << 10);
        bad |= (sc[i] >> 10) | ((s + tag) ^ h1);
      }
      if (__builtin_expect(__ballot(bad != 0u) == 0ull, 1)) break;
      if (++passes >= SPIN_MAX) { sc[0] = 99u; break; }
    }
    const u32 mx = max(max(sc[0], sc[1]), max(sc[2], sc[3]));
    if (__builtin_expect(__any(mx > CAP), 0)) { cnt[63] = 1; sc[0] = 0; sc[1] = 0; sc[2] = 0; sc[3] = 0; }
    auto take = [&](auto nw, int j0) {
      constexpr int NK = 4 * decltype(nw)::value;
      u32 rk[NK], nk[NK];
#pragma unroll
      for (int k = 0; k < NK; ++k) {
        const int i = k & 3, j = j0 + (k >> 2);
        nk[k] = (u32)(4 * j + part - 2) < sc[i] ? (q[i][j] & 0xfcu) : dum - L_CNT;
        rk[k] = atomicAdd((u32*)(lds + L_CNT + nk[k]), 1u);
      }
#pragma unroll
      for (int k = 0; k < NK; ++k) {
        const int i = k & 3, j = j0 + (k >> 2);
        lds_st<u32>(lds, L_BK + min(nk[k], 0xbcu) * 64 + (rk[k] & 63u) * 4, (q[i][j] & ~0xffu) | (u32)((yt >> 2) + 64 * i));
      }
    };
    while (lds_peek(lds, L_CNT + 53 * 4) < 4u) __builtin_amdgcn_s_sleep(1);
    asm volatile("" ::: "memory");
    if (__any(mx > 6u)) {
      take(IC<3>(), 0);
      if (__builtin_expect(__any(mx > 10u), 0)) take(IC<1>(), 3);
    } else {
      take(IC<2>(), 0);
    }
  } else {
    while (lds_peek(lds, L_CNT + 56 * 4) < 4u) __builtin_amdgcn_s_sleep(1);
    asm volatile("" ::: "memory");
    const int t = tid;
    gld2p(tb0, t < 128 ? we + 256 * 128 + t : pos + n0 * 3 + min(t - 128, 3 * nn - 1));
    gld2(tb1, we + 257 * 128, (u32)(t & 127) * 2u);
    gld2(tb2, wx, (u32)(t & 127) * 2u);
    stage_w(wh, L_ROWS);
    {
      const int wb = wrow + (((4 * wq + tp) ^ wkey) * 16);
      f32x4 a[3], c[3];
#pragma unroll
      for (int nt = 0; nt < 3; ++nt) { a[nt] = f32x4{0.f, 0.f, 0.f, 0.f}; c[nt] = a[nt]; }
#pragma unroll
      for (int s = 0; s < 4; ++s) {
        const bf16x8 w0 = cat4(lds_tr(lds, wb + s * 8192), lds_tr(lds, wb + s * 8192 + 1024));
        const bf16x8 w1 = cat4(lds_tr(lds, wb + 8 + s * 8192), lds_tr(lds, wb + 8 + s * 8192 + 1024));
#pragma unroll
        for (int nt = 0; nt < 3; ++nt) { const bf16x8 hb = hfrag(nt, s); a[nt] = MFMA16(w0, hb, a[nt]); c[nt] = MFMA16(w1, hb, c[nt]); }
      }
      asm volatile("s_nop 7" : "+v"(a[0]), "+v"(a[1]), "+v"(a[2]), "+v"(c[0]), "+v"(c[1]), "+v"(c[2]));
#pragma unroll
      for (int nt = 0; nt < 3; ++nt) {
        const u32x2 o0 = {scale2(a[nt][0], a[nt][1]), scale2(a[nt][2], a[nt][3])}, o1 = {scale2(c[nt][0], c[nt][1]), scale2(c[nt][2], c[nt][3])};
        const int ad = L_AIMG + (16 * nt + l15) * AP + 64 * wq + 16 * g4;
        lds_st<u32x2>(lds, ad, o0);
        lds_st<u32x2>(lds, ad + 8, o1);
      }
    }
    while (lds_peek(lds, L_CNT + 60 * 4) < 4u) __builtin_amdgcn_s_sleep(1);
    stage_w(wh + 128 * 128, L_ROWS + 32768);
    {
      const u32 o = (u32)(b >> 3) * 10000u + (u32)(wq * 2560 + lane * 16);
      dma1(ea, min(o, 319984u), ldsbase + L_JUNK);
      dma1(ea, min(o + 1024u, 319984u), ldsbase + L_JUNK);
      if (wq < 2) dma1(ea, min(o + 2048u + (lane >= 32 ? 4608u : 0u), 319984u), ldsbase + L_JUNK);
      else dma1(pos, min((u32)(b >> 3) * 1888u + (u32)((wq - 2) * 1024 + lane * 16), 59984u), ldsbase + L_JUNK);
    }
    asm volatile("s_waitcnt vmcnt(3)" : "+v"(tb0), "+v"(tb1), "+v"(tb2) :: "memory");
    if (w < 2) {
      const float wd = bf2f(tb0) * CL2, wq = bf2f(tb1) * CL2;
      const u32 hi = pk_bf16(wd, wq);
      const u32 lo = pk_bf16(wd - lo_f(hi), wq - hi_f(hi));
      lds_st<u16>(lds, L_AIMG + 48 * AP + t * 2, (u16)hi);
      lds_st<u16>(lds, L_AIMG + 49 * AP + t * 2, (u16)(hi >> 16));
      lds_st<u16>(lds, L_AIMG + 50 * AP + t * 2, (u16)lo);
      lds_st<u16>(lds, L_AIMG + 51 * AP + t * 2, (u16)(lo >> 16));
      lds_st<u16>(lds, L_AIMG + 52 * AP + t * 2, (u16)0);
      lds_st<float>(lds, L_WX + t * 4, bf2f(tb2));
    } else {
      const int i = (t - 128) / 3, c = t - 128 - 3 * i;
      lds_st<float>(lds, t < 128 + 3 * nn ? L_POS + i * 16 + c * 4 : dum, bf2f(tb0));
    }
  }
  __builtin_amdgcn_s_setprio(0);
  wg_barrier();
  if (w == 3) { gld16c(fl[0], ws + WS_FLAG, (u32)lane * 32u); gld16c(fl[1], ws + WS_FLAG, (u32)lane * 32u + 16u); }

  const u32 cl = cnt[lane];
  const u32 clm = lane < nn ? cl : 0u;
  u32 incl = clm;
  incl += __builtin_amdgcn_update_dpp(0u, incl, 0x111, 0xf, 0xf, false);
  incl += __builtin_amdgcn_update_dpp(0u, incl, 0x112, 0xf, 0xf, false);
  incl += __builtin_amdgcn_update_dpp(0u, incl, 0x114, 0xf, 0xf, false);
  incl += __builtin_amdgcn_update_dpp(0u, incl, 0x118, 0xf, 0xf, false);
  incl += __builtin_amdgcn_update_dpp(0u, incl, 0x142, 0xa, 0xf, false);
  incl += __builtin_amdgcn_update_dpp(0u, incl, 0x143, 0xc, 0xf, false);
  const u32 total = (u32)__builtin_amdgcn_readlane((int)incl, 63);
  const u32 excl = incl - clm;
  bool slow = (__builtin_amdgcn_readlane((int)cl, 63) != 0) | (total > LCAP) | (__ballot(clm > 64u) != 0) | (ldsbase != 0);
  const int myr = 2 * (w & 3) + (w >> 2);
  int g_n, g_end;
  {
    const int bal = min((int)((float)(2 * excl + clm) * (4.0f * __builtin_amdgcn_rcpf((float)max(total, 1u)))), 7);
    const int rid = lane < nn ? bal : 99 + lane;
    const int prev = __builtin_amdgcn_update_dpp(-1, rid, 0x138, 0xf, 0xf, false);
    unsigned long long same = __ballot(rid == prev);
    same &= same >> 1; same &= same >> 2; same &= same >> 4;
    g_n = __popcll(__ballot(rid < myr));
    g_end = __popcll(__ballot(rid <= myr));
    if (same) { g_n = 5 * myr; g_end = min(5 * myr + 5, nn); }
  }
  const int g_cnt = g_end - g_n;
  const int eb = slow ? 0 : __builtin_amdgcn_readlane((int)excl, g_n);
  int ee = slow ? 0 : __builtin_amdgcn_readlane((int)excl, g_end);
  const int lbase = L_LIST + (eb + 32 * myr) * 8;
  bf16x8 wh1[4], wh2[4];
  {
    const u32 pk = (excl - (u32)eb) | ((lane < g_end && !slow ? clm : 0u) << 16);
    u32 bk[4], oc[4];
#pragma unroll
    for (int i = 0; i < 4; ++i) {
      bk[i] = lds_ld<u32>(lds, L_BK + (g_n + hh) * 256 + r * 4 + i * 512);
      oc[i] = (u32)__builtin_amdgcn_ds_bpermute((g_n + hh + 2 * i) * 4, (int)pk);
    }
    const u32x2 zz = {0, 0};
    lds_st<u32x2>(lds, lbase + (ee - eb + r) * 8, zz);
#pragma unroll
    for (int s = 0; s < 4; ++s) {
      wh1[s] = cat4(lds_tr(lds, wa + s * 8192), lds_tr(lds, wa + s * 8192 + 1024));
      wh2[s] = cat4(lds_tr(lds, wa + 32768 + s * 8192), lds_tr(lds, wa + 32768 + s * 8192 + 1024));
    }
#pragma unroll
    for (int i = 0; i < 4; ++i) {
      const u32 nl = (u32)(g_n + hh + 2 * i);
      const u32x2 t = {bk[i] & 0x3fff00u, ((bk[i] & 0xffu) * (2 * CHUNK) + ((bk[i] >> 21) & 0x7feu)) | (nl << 19)};
      lds_st<u32x2>(lds, (u32)r < (oc[i] >> 16) ? lbase + ((oc[i] & 0xffffu) + r) * 8 : dum, t);
    }
    if (__builtin_expect(__any(clm > 32u), 0)) {
#pragma unroll
      for (int i = 0; i < 4; ++i) {
        const u32 nl = (u32)(g_n + hh + 2 * i);
        const u32 e2 = lds_ld<u32>(lds, L_BK + nl * 256 + (r + 32) * 4);
        const u32x2 t = {e2 & 0x3fff00u, ((e2 & 0xffu) * (2 * CHUNK) + ((e2 >> 21) & 0x7feu)) | (nl << 19)};
        lds_st<u32x2>(lds, (u32)(r + 32) < (oc[i] >> 16) ? lbase + ((oc[i] & 0xffffu) + r + 32) * 8 : dum, t);
      }
    }
  }
  u32x2 le = lds_ld<u32x2>(lds, lbase + r * 8);
  u32 vo[4];
#pragma unroll
  for (int pc = 0; pc < 4; ++pc) vo[pc] = lds_ld<u32>(lds, lbase + (lane >> 3) * 8 + pc * 64) | dc;
  u32x2 gp;
  u32 ge;
  if (w != 3 && lane < 32) { gld8(gp, pos, ((le[0] >> 8) * 6u) & ~3u); gld2hi(ge, ea, le[1] & 0x7ffffu); }
  if (w == 3) {
    int fp = 0;
    asm volatile("s_waitcnt vmcnt(0)" : "+v"(fl[0]), "+v"(fl[1]) :: "memory");
#pragma clang loop unroll(disable)
    while (__builtin_expect(!__all(((fl[0][0] ^ tag) | (fl[0][1] ^ ~tag) | (fl[0][2] ^ tag) | (fl[0][3] ^ ~tag) | (fl[1][0] ^ tag) |
                                    (fl[1][1] ^ ~tag) | (fl[1][2] ^ tag) | (fl[1][3] ^ ~tag)) == 0u), 0)) {
      if (++fp >= SPIN_MAX) { cnt[62] = 1; break; }
      gld16c2w(fl[0], fl[1], ws + WS_FLAG, (u32)lane * 32u, (u32)lane * 32u + 16u);
    }
    if (lane < 32) { gld8(gp, pos, ((le[0] >> 8) * 6u) & ~3u); gld2hi(ge, ea, le[1] & 0x7ffffu); }
  }
  asm volatile("" : "+v"(wh1[0]), "+v"(wh1[1]), "+v"(wh1[2]), "+v"(wh1[3]), "+v"(wh2[0]), "+v"(wh2[1]), "+v"(wh2[2]), "+v"(wh2[3]),
                    "+v"(vo[0]), "+v"(vo[1]), "+v"(vo[2]), "+v"(vo[3]));
  wg_barrier();
  const bool bp_ok = __builtin_amdgcn_readfirstlane((int)cnt[62]) == 0;
  slow = slow || !bp_ok;
  if (slow) ee = eb;
  if (!gx && ee > eb) dma_rows(ws + WS_BP, vo[0], vo[1], vo[2], vo[3], ldsbase + rowbuf);
  f32x4 z[3];
#pragma unroll
  for (int nt = 0; nt < 3; ++nt) z[nt] = f32x4{0.f, 0.f, 0.f, 0.f};
#pragma unroll
  for (int s = 0; s < 4; ++s)
#pragma unroll
    for (int nt = 0; nt < 3; ++nt) z[nt] = MFMA16(wh1[s], hfrag(nt, s), z[nt]);
  if (gx) {
    asm volatile("" : "+v"(z[0]), "+v"(z[1]), "+v"(z[2]));
    if (ee > eb) dma_rows(ws + WS_BP, vo[0], vo[1], vo[2], vo[3], ldsbase + rowbuf);
  }

  f32x16 acc[4];
  if (!slow) {
    bf16x8 ebf[4];
    {
      const int colo = (16 * (g4 & 1) + 4 * tp) * 2;
      const int a1 = L_AIMG + (hh ? 48 + tq : g_n + tq) * AP + colo;
      const int a2 = L_AIMG + (hh ? 52 : g_n + 4 + tq) * AP + colo;
#pragma unroll
      for (int ct = 0; ct < 4; ++ct) ebf[ct] = cat4(lds_tr(lds, a1 + 64 * ct), lds_tr(lds, a2 + 64 * ct));
    }
    u32 lptr = ldsbase + lbase + 256 + (lane >> 3) * 8, lep = ldsbase + lbase + 256 + r * 8;
    asm volatile(LOOP_ASM
                 : "=&v"(acc[0]), "=&v"(acc[1]), "=&v"(acc[2]), "=&v"(acc[3]), "+v"(lptr), "+v"(lep)
                 : "v"(id[0]), "v"(id[1]), "v"(ebf[0]), "v"(ebf[1]), "v"(ebf[2]), "v"(ebf[3]), "s"(ee - eb), "v"(boff0),
                   "v"(boff1), "v"(soff), "v"(eoff), "v"(dc), "v"(zad), "v"(swb - 2 * g_n), "v"(x2a), "s"(ws + WS_BP),
                   "s"(ldsbase + rowbuf), "s"(pos), "s"(ea), "v"(le[0]), "v"(le[1]), "v"(gp), "v"(ge), "i"(L_POS)
                 : LOOP_CLOBBERS);
  } else {
    VMWAIT(0);
    float* macc = (float*)(lds + L_ROWS);
    float* pacc = (float*)(lds + L_ROWS + 20480);
    for (int i = tid; i < 40 * 128 + 160; i += 512) macc[i] = 0.f;
    __syncthreads();
    const u32 wd2 = *(const u32*)(we + 256 * 128 + 2 * lane), wa2 = *(const u32*)(we + 257 * 128 + 2 * lane), wx2 = *(const u32*)(wx + 2 * lane);
    for (int e0 = w * 64; e0 < EE; e0 += 512) {
      const int dv = dstg[e0 + lane] - n0;
      unsigned long long m = __ballot((unsigned)dv < (unsigned)nn);
      while (m) {
        const int bit = __ffsll((long long)m) - 1;
        m &= m - 1;
        const int e = e0 + bit;
        const int nl = __builtin_amdgcn_readlane(dv, bit);
        const int s = srcg[e];
        float df[3], d2 = 0.f;
#pragma unroll
        for (int i = 0; i < 3; ++i) {
          df[i] = lo_f(pk_bf16(lds_ld<float>(lds, L_POS + nl * 16 + i * 4) - bf2f(pos[s * 3 + i]), 0.f));
          d2 += lo_f(pk_bf16(df[i] * df[i], 0.f));
        }
        d2 = lo_f(pk_bf16(d2, 0.f)) * CL2;
        const float eat = bf2f(ea[e]) * CL2;
        const u32 a2 = lds_ld<u32>(lds, L_AIMG + nl * AP + lane * 4);
        u32 b2;
        if (bp_ok) {
          b2 = __hip_atomic_load((u32*)(ws + WS_BP + (size_t)s * 256 + lane * 4), __ATOMIC_RELAXED, __HIP_MEMORY_SCOPE_AGENT);
        } else {
          float t0 = 0.f, t1 = 0.f;
          for (int k = 0; k < 128; ++k) {
            const float hv = bf2f(h[s * 128 + k]);
            const u32 wv = *(const u32*)(we + (128 + k) * 128 + 2 * lane);
            t0 += hv * lo_f(wv); t1 += hv * hi_f(wv);
          }
          b2 = pk_bf16(t0 * CL2, t1 * CL2);
        }
        const float m0 = lo_f(act1(lo_f(a2) + lo_f(b2) + d2 * lo_f(wd2) + eat * lo_f(wa2)));
        const float m1 = lo_f(act1(hi_f(a2) + hi_f(b2) + d2 * hi_f(wd2) + eat * hi_f(wa2)));
        atomicAdd(&macc[nl * 128 + 2 * lane], m0);
        atomicAdd(&macc[nl * 128 + 2 * lane + 1], m1);
        float t = m0 * lo_f(wx2) + m1 * hi_f(wx2);
#pragma unroll
        for (int o = 32; o > 0; o >>= 1) t += __shfl_xor(t, o);
        if (lane < 3) atomicAdd(&pacc[nl * 4 + lane], (lane == 0 ? df[0] : lane == 1 ? df[1] : df[2]) * t);
      }
    }
    __syncthreads();
    for (int i = tid; i < 40 * 64; i += 512) {
      const int nl = i >> 6, c2 = i & 63;
      lds_st<u32>(lds, L_MIMG + nl * MP + c2 * 4, pk_bf16(macc[nl * 128 + 2 * c2], macc[nl * 128 + 2 * c2 + 1]));
    }
    if (tid < 3 * nn) {
      const int i = tid / 3, cc = tid - 3 * i;
      out[NN * 128 + n0 * 3 + tid] = (u16)pk_bf16(lds_ld<float>(lds, L_POS + i * 16 + cc * 4) + pacc[i * 4 + cc], 0.f);
    }
#pragma unroll
    for (int ct = 0; ct < 4; ++ct)
#pragma unroll
      for (int i = 0; i < 16; ++i) acc[ct][i] = 0.f;
  }
  if (tid == 0) __hip_atomic_fetch_add((u32*)(ws + WS_GEN), 1u, __ATOMIC_RELAXED, __HIP_MEMORY_SCOPE_AGENT);
  int tl = tid;
  asm volatile("" : "+v"(tl));
  const int t15 = tl & 15, t4 = (tl >> 4) & 3, th = (tl >> 5) & 1, comp = (tl >> 3) & 3, nlr = tl & 7;
  u32x2 hres[3];
  f32x4 wv[16];
#pragma unroll
  for (int nt = 0; nt < 3; ++nt) hres[nt] = lds_ld<u32x2>(lds, L_HT + t15 * 256 + nt * 4096 + (((2 * w + (t4 >> 1)) ^ t15) * 16) + (t4 & 1) * 8);
#pragma unroll
  for (int k = 0; k < 16; ++k) wv[k] = lds_ld<f32x4>(lds, L_WX + 16 * th + 32 * k);
  float p0 = lds_ld<float>(lds, L_POS + min(g_n + nlr, 39) * 16 + max(comp - 1, 0) * 4);
  __builtin_amdgcn_sched_barrier(0);
  if (!slow && comp == 0 && nlr < g_cnt) {
    const u32 ad = (u32)(L_MIMG + (g_n + nlr) * MP + 8 * th);
#pragma unroll
    for (int k = 0; k < 16; ++k)
      lds_st<u32x2>(lds, ad + 16 * k, u32x2{pk_bf16(acc[k >> 2][4 * (k & 3)], acc[k >> 2][4 * (k & 3) + 1]),
                                            pk_bf16(acc[k >> 2][4 * (k & 3) + 2], acc[k >> 2][4 * (k & 3) + 3])});
  }
  u32 pad = (comp > 0 && th == 0 && nlr < g_cnt && !slow) ? (u32)(L_S + 16384 + ((g_n + nlr) * 3 + comp - 1) * 2) : (u32)(L_DUM + (tl & 63) * 8);
  u32 mad = (u32)(L_MIMG + t15 * MP + 16 * t4), oad = (u32)(L_S + t15 * MP + (16 * w + 4 * t4) * 2), rad = (u32)(L_S + (tl >> 4) * MP + t15 * 16);
  asm volatile("" : "+v"(pad), "+v"(mad), "+v"(oad), "+v"(rad));
  wg_barrier();

  {
    bf16x8 mb[4][3];
#pragma unroll
    for (int s = 0; s < 4; ++s)
#pragma unroll
      for (int nt = 0; nt < 3; ++nt) mb[s][nt] = lds_ld<bf16x8>(lds, mad + nt * 16 * MP + 64 * s);
    __builtin_amdgcn_sched_barrier(0);
    f32x2 t0 = {0.f, 0.f}, t1 = {0.f, 0.f};
#pragma unroll
    for (int k = 0; k < 16; ++k) {
      t0 = __builtin_elementwise_fma(f32x2{acc[k >> 2][4 * (k & 3)], acc[k >> 2][4 * (k & 3) + 1]}, f32x2{wv[k][0], wv[k][1]}, t0);
      t1 = __builtin_elementwise_fma(f32x2{acc[k >> 2][4 * (k & 3) + 2], acc[k >> 2][4 * (k & 3) + 3]}, f32x2{wv[k][2], wv[k][3]}, t1);
    }
    t0 += t1;
    const float u = t0[0] + t0[1];
    const float pnew = p0 + u + __shfl_xor(u, 32);
    __builtin_amdgcn_sched_barrier(0);
#pragma unroll
    for (int s = 0; s < 4; ++s)
#pragma unroll
      for (int nt = 0; nt < 3; ++nt) z[nt] = MFMA16(wh2[s], mb[s][nt], z[nt]);
    __builtin_amdgcn_sched_barrier(0);
#pragma unroll
    for (int nt = 0; nt < 3; ++nt) {
      u32x2 ov;
#pragma unroll
      for (int i = 0; i < 2; ++i) {
        const f32x2 zz = {z[nt][2 * i], z[nt][2 * i + 1]};
        const f32x2 y = zz * CL2;
        const f32x2 d = f32x2{__builtin_amdgcn_exp2f(y[0]), __builtin_amdgcn_exp2f(y[1])} + 1.f;
        const f32x2 sg = {__builtin_amdgcn_rcpf(d[0]), __builtin_amdgcn_rcpf(d[1])};
        const f32x2 o = __builtin_elementwise_fma(zz, sg, f32x2{lo_f(hres[nt][i]), hi_f(hres[nt][i])});
        ov[i] = pk_bf16(o[0], o[1]);
      }
      lds_st<u32x2>(lds, oad + nt * 16 * MP, ov);
    }
    lds_st<u16>(lds, pad, (u16)pk_bf16(pnew, 0.f));
    wg_barrier();
    asm volatile(".Legnn_code_end:");
    const u32x4 o0 = lds_ld<u32x4>(lds, rad);
    if (w < 2) {
      const u32x4 o1 = lds_ld<u32x4>(lds, rad + 32 * MP);
      if (tl + 512 < nn * 16) gst16(out, (u32)(n0 * 256 + 8192 + tl * 16), o1);
    } else if (w < 4) {
      const u16 pv = lds_ld<u16>(lds, L_S + 16384 + (tl - 128) * 2);
      if (!slow && tl - 128 < 3 * nn) out[NN * 128 + n0 * 3 + tl - 128] = pv;
    }
    gst16(out, (u32)(n0 * 256 + tl * 16), o0);
  }
}


__global__ void k_gen_pre(const u16* h, const u16* we, float* ab) {
  __shared__ float hs[128];
  const int n = blockIdx.x, c = threadIdx.x;
  if (c < 128) hs[c] = bf2f(h[(size_t)n * 128 + c]);
  __syncthreads();
  float a = 0.f;
  for (int k = 0; k < 128; ++k) a += hs[k] * bf2f(we[((c >> 7) * 128 + k) * 128 + (c & 127)]);
  ab[(size_t)n * 256 + c] = a;
}
__global__ void k_gen_edge(const float* ab, const u16* pos, const u16* ea, const int* src, const int* dst, const u16* we,
                           const u16* wx, float* mi, float* pacc, int ne) {
  const int e = blockIdx.x * 4 + (threadIdx.x >> 6), l = threadIdx.x & 63;
  if (e >= ne) return;
  const int s = src[e], d = dst[e];
  float df[3], d2 = 0.f, t = 0.f;
  for (int i = 0; i < 3; ++i) {
    df[i] = lo_f(pk_bf16(bf2f(pos[d * 3 + i]) - bf2f(pos[s * 3 + i]), 0.f));
    d2 += lo_f(pk_bf16(df[i] * df[i], 0.f));
  }
  d2 = lo_f(pk_bf16(d2, 0.f));
  for (int j = 0; j < 2; ++j) {
    const int c = l + 64 * j;
    const float x = ab[(size_t)d * 256 + c] + ab[(size_t)s * 256 + 128 + c] + d2 * bf2f(we[256 * 128 + c]) + bf2f(ea[e]) * bf2f(we[257 * 128 + c]);
    const float m = lo_f(act1(x * CL2));
    atomicAdd(&mi[(size_t)d * 128 + c], m);
    t += m * bf2f(wx[c]);
  }
  for (int o = 32; o > 0; o >>= 1) t += __shfl_xor(t, o);
  if (l < 3) atomicAdd(&pacc[(size_t)d * 4 + l], (l == 0 ? df[0] : l == 1 ? df[1] : df[2]) * t);
}
__global__ void k_gen_node(const u16* h, const u16* pos, const float* mi, const float* pacc, const u16* wh, u16* out, int nn) {
  __shared__ float xs[256];
  const int n = blockIdx.x, c = threadIdx.x;
  xs[c] = bf2f(h[(size_t)n * 128 + c]);
  xs[128 + c] = lo_f(pk_bf16(mi[(size_t)n * 128 + c], 0.f));
  __syncthreads();
  float a = 0.f;
  for (int k = 0; k < 256; ++k) a += xs[k] * bf2f(wh[k * 128 + c]);
  out[(size_t)n * 128 + c] = (u16)pk_bf16(xs[c] + a * __builtin_amdgcn_rcpf(1.f + __builtin_amdgcn_exp2f(a * CL2)), 0.f);
  if (c < 3) out[(size_t)nn * 128 + n * 3 + c] = (u16)pk_bf16(bf2f(pos[n * 3 + c]) + pacc[(size_t)n * 4 + c], 0.f);
}

extern "C" void kernel_launch(void* const* d_in, const int* in_sizes, int n_in, void* d_out, int out_size, void* d_ws,
                              size_t ws_size, hipStream_t stream) {
  const u16 *h = (const u16*)d_in[0], *pos = (const u16*)d_in[1], *ea = (const u16*)d_in[2];
  const int *src = (const int*)d_in[3], *dst = (const int*)d_in[4];
  const u16 *we = (const u16*)d_in[5], *wx = (const u16*)d_in[6], *wh = (const u16*)d_in[7];
  const int n = in_sizes[0] / 128, e = in_sizes[3];
  if (n == NN && e == EE) {
    k_egnn<<<NBIN, 512, 0, stream>>>(h, pos, ea, src, dst, we, wx, wh, (char*)d_ws, (u16*)d_out);
  } else {
    float* ab = (float*)d_ws;
    float* mi = ab + (size_t)n * 256;
    float* pacc = mi + (size_t)n * 128;
    (void)hipMemsetAsync(mi, 0, (size_t)n * 132 * 4, stream);
    k_gen_pre<<<n, 256, 0, stream>>>(h, we, ab);
    k_gen_edge<<<(e + 3) / 4, 256, 0, stream>>>(ab, pos, ea, src, dst, we, wx, mi, pacc, e);
    k_gen_node<<<n, 128, 0, stream>>>(h, pos, mi, pacc, wh, (u16*)d_out, n);
  }
}
```
